```python
import torch

def _softmax(x, axis=-1):
    return torch.softmax(x, dim=axis)
def _gelu(x):
    return torch.nn.functional.gelu(x, approximate="tanh")
def _silu(x):
    return torch.nn.functional.silu(x)
def _sigmoid(x):
    return torch.sigmoid(x)
def _layernorm(x, axis=-1, eps=1e-5):
    m = x.mean(dim=axis, keepdim=True)
    v = x.var(dim=axis, keepdim=True, unbiased=False)
    return (x - m) / torch.sqrt(v + eps)
def _scatter_add(tgt, idx, src, axis=0):
    return tgt.clone().index_add_(axis, idx, src)
def _segment_sum(src, seg, n):
    out = torch.zeros((n,) + src.shape[1:], dtype=src.dtype)
    return out.index_add_(0, seg, src)
def _pad(x, pw):
    flat = tuple(v for pair in reversed(pw) for v in pair)
    return torch.nn.functional.pad(x, flat)

kernel_name = 'parallel_sum__gqa_block_cached__geglu_ffn__decode'

def setup_inputs():

    _w = (lambda a: (lambda t: t.to(torch.bfloat16) if t.is_floating_point() else t)(torch.as_tensor(a)))
    def _A_setup():
        B, Sq, D = 2, 1, 640
        Hq, Hkv, Sk, Dh = 32, 32, 4096, 128
        assert Hq % Hkv == 0, (Hq, Hkv)
        return {
            "x": _w((lambda *s: torch.randn(*s, dtype=torch.bfloat16) * 0.05)(B, Sq, D)),
            "k_cache": _w((lambda *s: torch.randn(*s, dtype=torch.bfloat16) * 3.2)(B, Hkv, Sk, Dh)),
            "v_cache": _w((lambda *s: torch.randn(*s, dtype=torch.bfloat16) * 1.6)(B, Hkv, Sk, Dh)),
            "wq": _w((lambda *s: torch.randn(*s, dtype=torch.bfloat16) * 3.2)(D, Hq * Dh) * (D ** -0.5)),
            "wo": _w((lambda *s: torch.randn(*s, dtype=torch.bfloat16) * 1.6)(Hq * Dh, D) * ((Hq * Dh) ** -0.5)),
        }
    _A_in = _A_setup()

    def _B_setup():
        B, S, D, F = 2, 1, 640, 1728
        return {
            "x": _w((lambda *s: torch.randn(*s, dtype=torch.bfloat16) * 0.05)(B, S, D)),
            "w_gate": _w((lambda *s: torch.randn(*s, dtype=torch.bfloat16) * 0.05)(D, F)),
            "w_up": _w((lambda *s: torch.randn(*s, dtype=torch.bfloat16) * 0.05)(D, F)),
            "w_down": _w((lambda *s: torch.randn(*s, dtype=torch.bfloat16) * 0.05)(F, D)),
        }
    _B_in = _B_setup()

    return {
        "A_x": _A_in["x"],
        "A_k_cache": _A_in["k_cache"],
        "A_v_cache": _A_in["v_cache"],
        "A_wq": _A_in["wq"],
        "A_wo": _A_in["wo"],
        "B_x": _B_in["x"],
        "B_w_gate": _B_in["w_gate"],
        "B_w_up": _B_in["w_up"],
        "B_w_down": _B_in["w_down"],
    }

def reference(A_x, A_k_cache, A_v_cache, A_wq, A_wo, B_x, B_w_gate, B_w_up, B_w_down):
    def _A_ref(x, k_cache, v_cache, wq, wo):
        B, Sq, D = x.shape
        Hq, Dh = 32, 128
        Hkv = k_cache.shape[1]
        rep = Hq // Hkv
        q = (lambda x, *d: x.permute(*d))((x @ wq).reshape(B, Sq, Hq, Dh), 0, 2, 1, 3)
        k = (lambda x, n, axis: torch.repeat_interleave(x, n, dim=axis))(k_cache, rep, axis=1)
        v = (lambda x, n, axis: torch.repeat_interleave(x, n, dim=axis))(v_cache, rep, axis=1)
        scores = torch.einsum("bhqd,bhkd->bhqk", q, k) * (Dh ** -0.5)
        p = _softmax(scores, axis=-1)
        o = torch.einsum("bhqk,bhkd->bhqd", p, v)
        return (lambda x, *d: x.permute(*d))(o, 0, 2, 1, 3).reshape(B, Sq, Hq * Dh) @ wo

    def _B_ref(x, w_gate, w_up, w_down):
        return (_gelu(x @ w_gate) * (x @ w_up)) @ w_down

    x = A_x
    xn = _layernorm(x, axis=-1)
    return x + _A_ref(xn, A_k_cache, A_v_cache, A_wq, A_wo) + _B_ref(xn, B_w_gate, B_w_up, B_w_down)
```

```cpp
#include <hip/hip_runtime.h>
#include <hip/hip_bf16.h>
#include <stdint.h>

typedef unsigned u32x4 __attribute__((ext_vector_type(4)));
typedef __bf16 bf16x2 __attribute__((ext_vector_type(2)));
typedef unsigned short u16;

constexpr int B = 2, D = 640, H = 32, SK = 4096, DH = 128, F = 1728;
constexpr int NSPLIT = 8;
constexpr int KPS = SK / NSPLIT;

__device__ __forceinline__ float bf2f(u16 v) { return __uint_as_float(((unsigned)v) << 16); }
__device__ __forceinline__ float lo16(unsigned v) { return __uint_as_float(v << 16); }
__device__ __forceinline__ float hi16(unsigned v) { return __uint_as_float(v & 0xffff0000u); }
__device__ __forceinline__ float rbf(float x) { return __bfloat162float(__float2bfloat16(x)); }
__device__ __forceinline__ float dot2(unsigned k, unsigned q, float acc) {
  return __builtin_amdgcn_fdot2_f32_bf16(__builtin_bit_cast(bf16x2, k), __builtin_bit_cast(bf16x2, q), acc, false);
}

__device__ void layernorm_to_lds(const u16* x, float* xn  , float* red) {
  int tid = threadIdx.x, lane = tid & 63, w = tid >> 6;
  if (w < 2) {
    float v[10]; float s = 0.f;
    for (int i = 0; i < 10; ++i) { v[i] = bf2f(x[w * D + lane + 64 * i]); s += v[i]; }
    for (int o = 32; o; o >>= 1) s += __shfl_xor(s, o);
    float m = s / D; float q = 0.f;
    for (int i = 0; i < 10; ++i) { float d = v[i] - m; q += d * d; }
    for (int o = 32; o; o >>= 1) q += __shfl_xor(q, o);
    float var = q / D;
    float mb = rbf(m), vb = rbf(var);
    float den = rbf(sqrtf(rbf(vb + 1e-5f)));
    for (int i = 0; i < 10; ++i) xn[w * D + lane + 64 * i] = rbf(rbf(v[i] - mb) / den);
  }
  __syncthreads();
}

__global__ void __launch_bounds__(256) k1(const u16* x, const u16* wq, const u16* wg, const u16* wu, float* q, float* hid) {
  __shared__ float xn[2 * D]; __shared__ float red[4 * 2 * 2 * 64 * 8];
  layernorm_to_lds(x, xn, red);
  int tid = threadIdx.x;
  int c8 = tid & 7, r = tid >> 3;
  if (blockIdx.x < 64) {
    int col0 = blockIdx.x * 64 + c8 * 8;
    float a0[8] = {0}, a1[8] = {0};
    for (int i = 0; i < 20; ++i) {
      int row = r + 32 * i;
      u32x4 w = *(const u32x4*)(wq + (size_t)row * 4096 + col0);
      float x0 = xn[row], x1 = xn[D + row];
      for (int k = 0; k < 4; ++k) {
        a0[2*k] += x0 * lo16(w[k]); a0[2*k+1] += x0 * hi16(w[k]);
        a1[2*k] += x1 * lo16(w[k]); a1[2*k+1] += x1 * hi16(w[k]);
      }
    }
    for (int k = 0; k < 8; ++k) for (int o = 8; o < 64; o <<= 1) { a0[k] += __shfl_xor(a0[k], o); a1[k] += __shfl_xor(a1[k], o); }
    int lane = tid & 63, w = tid >> 6;
    if (lane < 8) for (int k = 0; k < 8; ++k) { red[(w * 2 + 0) * 64 + lane * 8 + k] = a0[k]; red[(w * 2 + 1) * 64 + lane * 8 + k] = a1[k]; }
    __syncthreads();
    if (tid < 128) { int b = tid >> 6, c = tid & 63; float s = 0; for (int w2 = 0; w2 < 4; ++w2) s += red[(w2 * 2 + b) * 64 + c];
      q[b * 4096 + blockIdx.x * 64 + c] = rbf(s); }
  } else {
    int blk = blockIdx.x - 64;
    int col0 = blk * 64 + c8 * 8;
    float g0[8] = {0}, g1[8] = {0}, u0[8] = {0}, u1[8] = {0};
    for (int i = 0; i < 20; ++i) {
      int row = r + 32 * i;
      u32x4 w = *(const u32x4*)(wg + (size_t)row * F + col0);
      u32x4 v = *(const u32x4*)(wu + (size_t)row * F + col0);
      float x0 = xn[row], x1 = xn[D + row];
      for (int k = 0; k < 4; ++k) {
        g0[2*k] += x0 * lo16(w[k]); g0[2*k+1] += x0 * hi16(w[k]);
        g1[2*k] += x1 * lo16(w[k]); g1[2*k+1] += x1 * hi16(w[k]);
        u0[2*k] += x0 * lo16(v[k]); u0[2*k+1] += x0 * hi16(v[k]);
        u1[2*k] += x1 * lo16(v[k]); u1[2*k+1] += x1 * hi16(v[k]);
      }
    }
    for (int k = 0; k < 8; ++k) for (int o = 8; o < 64; o <<= 1) { g0[k] += __shfl_xor(g0[k], o); g1[k] += __shfl_xor(g1[k], o); u0[k] += __shfl_xor(u0[k], o); u1[k] += __shfl_xor(u1[k], o); }
    int lane = tid & 63, w = tid >> 6;
    if (lane < 8) for (int k = 0; k < 8; ++k) {
      red[(w * 4 + 0) * 64 + lane * 8 + k] = g0[k]; red[(w * 4 + 1) * 64 + lane * 8 + k] = g1[k];
      red[(w * 4 + 2) * 64 + lane * 8 + k] = u0[k]; red[(w * 4 + 3) * 64 + lane * 8 + k] = u1[k]; }
    __syncthreads();
    if (tid < 128) { int b = tid >> 6, c = tid & 63; float g = 0, u = 0; for (int w2 = 0; w2 < 4; ++w2) { g += red[(w2 * 4 + b) * 64 + c]; u += red[(w2 * 4 + 2 + b) * 64 + c]; }
      g = rbf(g); u = rbf(u);
      float t = tanhf(0.7978845608028654f * (g + 0.044715f * g * g * g));
      float ge = rbf(0.5f * g * (1.f + t));
      hid[b * F + blk * 64 + c] = rbf(ge * u); }
  }
}

__global__ void __launch_bounds__(256) k2(const float* q, const u16* K, const u16* V, float* pml, float* po) {
  __shared__ float sc[KPS]; __shared__ float red[4 * 128 + 16];
  int s = blockIdx.x, h = blockIdx.y, b = blockIdx.z;
  int tid = threadIdx.x, lane = tid & 63, w = tid >> 6, r = lane >> 4, c = lane & 15;
  const u16* kb = K + ((size_t)(b * H + h) * SK + s * KPS) * DH;
  const u16* vb = V + ((size_t)(b * H + h) * SK + s * KPS) * DH;
  float qf[8];
  for (int k = 0; k < 8; ++k) qf[k] = q[b * 4096 + h * 128 + c * 8 + k];
  for (int i = 0; i < KPS / 4 / 4; ++i) {
    int key = w * (KPS / 4) + i * 4 + r;
    u32x4 kv = __builtin_nontemporal_load((const u32x4*)(kb + (size_t)key * DH + c * 8));
    float d = 0;
    for (int k = 0; k < 4; ++k) { d += qf[2*k] * lo16(kv[k]) + qf[2*k+1] * hi16(kv[k]); }
    for (int o = 1; o < 16; o <<= 1) d += __shfl_xor(d, o);
    if (c == 0) sc[key] = d * 0.08838834764831845f;
  }
  __syncthreads();
  float m = -INFINITY;
  for (int i = tid; i < KPS; i += 256) m = fmaxf(m, sc[i]);
  for (int o = 32; o; o >>= 1) m = fmaxf(m, __shfl_xor(m, o));
  if (lane == 0) red[512 + w] = m;
  __syncthreads();
  m = fmaxf(fmaxf(red[512], red[513]), fmaxf(red[514], red[515]));
  float l = 0;
  for (int i = tid; i < KPS; i += 256) { float p = __expf(sc[i] - m); sc[i] = p; l += p; }
  for (int o = 32; o; o >>= 1) l += __shfl_xor(l, o);
  if (lane == 0) red[516 + w] = l;
  __syncthreads();
  l = red[516] + red[517] + red[518] + red[519];
  float acc[8] = {0};
  for (int i = 0; i < KPS / 4 / 4; ++i) {
    int key = w * (KPS / 4) + i * 4 + r;
    u32x4 vv = __builtin_nontemporal_load((const u32x4*)(vb + (size_t)key * DH + c * 8));
    float p = sc[key];
    for (int k = 0; k < 4; ++k) { acc[2*k] += p * lo16(vv[k]); acc[2*k+1] += p * hi16(vv[k]); }
  }
  for (int k = 0; k < 8; ++k) { acc[k] += __shfl_xor(acc[k], 16); acc[k] += __shfl_xor(acc[k], 32); }
  if (r == 0) for (int k = 0; k < 8; ++k) red[w * 128 + c * 8 + k] = acc[k];
  __syncthreads();
  int slot = (b * H + h) * NSPLIT + s;
  if (tid < 128) po[slot * 128 + tid] = red[tid] + red[128 + tid] + red[256 + tid] + red[384 + tid];
  if (tid == 0) { pml[slot * 2] = m; pml[slot * 2 + 1] = l; }
}

__global__ void __launch_bounds__(256) k3(const u16* x, const float* pml, const float* po, const float* hid, const u16* wo, const u16* wd, __hip_bfloat16* out) {
  __shared__ float o[2 * 4096]; __shared__ float hs[2 * F]; __shared__ float wgt[64 * NSPLIT]; __shared__ float red[4 * 32];
  int tid = threadIdx.x;
  if (tid < 64) { float M = -INFINITY; for (int s = 0; s < NSPLIT; ++s) M = fmaxf(M, pml[(tid * NSPLIT + s) * 2]);
    float L = 0; for (int s = 0; s < NSPLIT; ++s) L += __expf(pml[(tid * NSPLIT + s) * 2] - M) * pml[(tid * NSPLIT + s) * 2 + 1];
    for (int s = 0; s < NSPLIT; ++s) wgt[tid * NSPLIT + s] = __expf(pml[(tid * NSPLIT + s) * 2] - M) / L; }
  for (int i = tid; i < 2 * F; i += 256) hs[i] = hid[i];
  __syncthreads();
  for (int i = tid; i < 8192; i += 256) { int bh = i >> 7, d = i & 127; float a = 0; for (int s = 0; s < NSPLIT; ++s) a += wgt[bh * NSPLIT + s] * po[(bh * NSPLIT + s) * 128 + d]; o[i] = rbf(a); }
  __syncthreads();
  int col0 = blockIdx.x * 8;
  float a0[8] = {0}, a1[8] = {0}, f0[8] = {0}, f1[8] = {0};
  for (int row = tid; row < 4096; row += 256) {
    u32x4 w = *(const u32x4*)(wo + (size_t)row * D + col0);
    float x0 = o[row], x1 = o[4096 + row];
    for (int k = 0; k < 4; ++k) { a0[2*k] += x0 * lo16(w[k]); a0[2*k+1] += x0 * hi16(w[k]); a1[2*k] += x1 * lo16(w[k]); a1[2*k+1] += x1 * hi16(w[k]); }
  }
  for (int row = tid; row < F; row += 256) {
    u32x4 w = *(const u32x4*)(wd + (size_t)row * D + col0);
    float x0 = hs[row], x1 = hs[F + row];
    for (int k = 0; k < 4; ++k) { f0[2*k] += x0 * lo16(w[k]); f0[2*k+1] += x0 * hi16(w[k]); f1[2*k] += x1 * lo16(w[k]); f1[2*k+1] += x1 * hi16(w[k]); }
  }
  int lane = tid & 63, w = tid >> 6;
  for (int k = 0; k < 8; ++k) for (int of = 1; of < 64; of <<= 1) { a0[k] += __shfl_xor(a0[k], of); a1[k] += __shfl_xor(a1[k], of); f0[k] += __shfl_xor(f0[k], of); f1[k] += __shfl_xor(f1[k], of); }
  if (lane == 0) for (int k = 0; k < 8; ++k) { red[w * 32 + k] = a0[k]; red[w * 32 + 8 + k] = a1[k]; red[w * 32 + 16 + k] = f0[k]; red[w * 32 + 24 + k] = f1[k]; }
  __syncthreads();
  if (tid < 16) { int b = tid >> 3, k = tid & 7; float a = 0, f = 0; for (int w2 = 0; w2 < 4; ++w2) { a += red[w2 * 32 + b * 8 + k]; f += red[w2 * 32 + 16 + b * 8 + k]; }
    float xv = bf2f(x[b * D + col0 + k]);
    out[b * D + col0 + k] = __float2bfloat16(rbf(xv + rbf(a)) + rbf(f)); }
}

extern "C" void kernel_launch(void* const* d_in, const int* in_sizes, int n_in, void* d_out, int out_size, void* d_ws, size_t ws_size, hipStream_t stream) {
  const u16* x = (const u16*)d_in[0]; const u16* K = (const u16*)d_in[1]; const u16* V = (const u16*)d_in[2];
  const u16* wq = (const u16*)d_in[3]; const u16* wo = (const u16*)d_in[4];
  const u16* wg = (const u16*)d_in[6]; const u16* wu = (const u16*)d_in[7]; const u16* wd = (const u16*)d_in[8];
  float* ws = (float*)d_ws;
  float* q = ws; float* hid = ws + 8192; float* pml = ws + 16384; float* po = ws + 32768;
  k1<<<64 + 27, 256, 0, stream>>>(x, wq, wg, wu, q, hid);
  k2<<<dim3(NSPLIT, H, B), 256, 0, stream>>>(q, K, V, pml, po);
  k3<<<80, 256, 0, stream>>>(x, pml, po, hid, wo, wd, (__hip_bfloat16*)d_out);
}
```

```cpp
#define AUXKV 0
#include <hip/hip_runtime.h>
#include <hip/hip_bf16.h>
#include <stdint.h>

typedef unsigned u32x4 __attribute__((ext_vector_type(4)));
typedef __bf16 bf16x2 __attribute__((ext_vector_type(2)));
typedef unsigned short u16;

constexpr int B = 2, D = 640, H = 32, SK = 4096, DH = 128, F = 1728;
constexpr int NSPLIT = 4;
constexpr int KPS = SK / NSPLIT;

__device__ __forceinline__ float bf2f(u16 v) { return __uint_as_float(((unsigned)v) << 16); }
__device__ __forceinline__ float lo16(unsigned v) { return __uint_as_float(v << 16); }
__device__ __forceinline__ float hi16(unsigned v) { return __uint_as_float(v & 0xffff0000u); }
__device__ __forceinline__ float rbf(float x) { return __bfloat162float(__float2bfloat16(x)); }
__device__ __forceinline__ float dot2(unsigned k, unsigned q, float acc) {
  return __builtin_amdgcn_fdot2_f32_bf16(__builtin_bit_cast(bf16x2, k), __builtin_bit_cast(bf16x2, q), acc, false);
}

__device__ void layernorm_to_lds(const u16* x, float* xn  , float* red) {
  int tid = threadIdx.x, lane = tid & 63, w = tid >> 6;
  if (w < 2) {
    float v[10]; float s = 0.f;
    for (int i = 0; i < 10; ++i) { v[i] = bf2f(x[w * D + lane + 64 * i]); s += v[i]; }
    for (int o = 32; o; o >>= 1) s += __shfl_xor(s, o);
    float m = s / D; float q = 0.f;
    for (int i = 0; i < 10; ++i) { float d = v[i] - m; q += d * d; }
    for (int o = 32; o; o >>= 1) q += __shfl_xor(q, o);
    float var = q / D;
    float mb = rbf(m), vb = rbf(var);
    float den = rbf(sqrtf(rbf(vb + 1e-5f)));
    for (int i = 0; i < 10; ++i) xn[w * D + lane + 64 * i] = rbf(rbf(v[i] - mb) / den);
  }
  __syncthreads();
}

__global__ void __launch_bounds__(256) k1(const u16* x, const u16* wq, const u16* wg, const u16* wu, float* q, float* hid) {
  __shared__ float xn[2 * D]; __shared__ float red[4 * 2 * 2 * 64 * 8];
  layernorm_to_lds(x, xn, red);
  int tid = threadIdx.x;
  int c8 = tid & 7, r = tid >> 3;
  if (blockIdx.x < 64) {
    int col0 = blockIdx.x * 64 + c8 * 8;
    float a0[8] = {0}, a1[8] = {0};
    for (int i = 0; i < 20; ++i) {
      int row = r + 32 * i;
      u32x4 w = *(const u32x4*)(wq + (size_t)row * 4096 + col0);
      float x0 = xn[row], x1 = xn[D + row];
      for (int k = 0; k < 4; ++k) {
        a0[2*k] += x0 * lo16(w[k]); a0[2*k+1] += x0 * hi16(w[k]);
        a1[2*k] += x1 * lo16(w[k]); a1[2*k+1] += x1 * hi16(w[k]);
      }
    }
    for (int k = 0; k < 8; ++k) for (int o = 8; o < 64; o <<= 1) { a0[k] += __shfl_xor(a0[k], o); a1[k] += __shfl_xor(a1[k], o); }
    int lane = tid & 63, w = tid >> 6;
    if (lane < 8) for (int k = 0; k < 8; ++k) { red[(w * 2 + 0) * 64 + lane * 8 + k] = a0[k]; red[(w * 2 + 1) * 64 + lane * 8 + k] = a1[k]; }
    __syncthreads();
    if (tid < 128) { int b = tid >> 6, c = tid & 63; float s = 0; for (int w2 = 0; w2 < 4; ++w2) s += red[(w2 * 2 + b) * 64 + c];
      q[b * 4096 + blockIdx.x * 64 + c] = rbf(s); }
  } else {
    int blk = blockIdx.x - 64;
    int col0 = blk * 64 + c8 * 8;
    float g0[8] = {0}, g1[8] = {0}, u0[8] = {0}, u1[8] = {0};
    for (int i = 0; i < 20; ++i) {
      int row = r + 32 * i;
      u32x4 w = *(const u32x4*)(wg + (size_t)row * F + col0);
      u32x4 v = *(const u32x4*)(wu + (size_t)row * F + col0);
      float x0 = xn[row], x1 = xn[D + row];
      for (int k = 0; k < 4; ++k) {
        g0[2*k] += x0 * lo16(w[k]); g0[2*k+1] += x0 * hi16(w[k]);
        g1[2*k] += x1 * lo16(w[k]); g1[2*k+1] += x1 * hi16(w[k]);
        u0[2*k] += x0 * lo16(v[k]); u0[2*k+1] += x0 * hi16(v[k]);
        u1[2*k] += x1 * lo16(v[k]); u1[2*k+1] += x1 * hi16(v[k]);
      }
    }
    for (int k = 0; k < 8; ++k) for (int o = 8; o < 64; o <<= 1) { g0[k] += __shfl_xor(g0[k], o); g1[k] += __shfl_xor(g1[k], o); u0[k] += __shfl_xor(u0[k], o); u1[k] += __shfl_xor(u1[k], o); }
    int lane = tid & 63, w = tid >> 6;
    if (lane < 8) for (int k = 0; k < 8; ++k) {
      red[(w * 4 + 0) * 64 + lane * 8 + k] = g0[k]; red[(w * 4 + 1) * 64 + lane * 8 + k] = g1[k];
      red[(w * 4 + 2) * 64 + lane * 8 + k] = u0[k]; red[(w * 4 + 3) * 64 + lane * 8 + k] = u1[k]; }
    __syncthreads();
    if (tid < 128) { int b = tid >> 6, c = tid & 63; float g = 0, u = 0; for (int w2 = 0; w2 < 4; ++w2) { g += red[(w2 * 4 + b) * 64 + c]; u += red[(w2 * 4 + 2 + b) * 64 + c]; }
      g = rbf(g); u = rbf(u);
      float t = tanhf(0.7978845608028654f * (g + 0.044715f * g * g * g));
      float ge = rbf(0.5f * g * (1.f + t));
      hid[b * F + blk * 64 + c] = rbf(ge * u); }
  }
}

#define WIN 8
typedef __amdgpu_buffer_rsrc_t rsrc_t;
__device__ __forceinline__ rsrc_t mk(const void* p, unsigned bytes) { return __builtin_amdgcn_make_buffer_rsrc((void*)p, 0, bytes, 0x00020000); }
template <int AUX> __device__ __forceinline__ u32x4 bl(rsrc_t r, int voff, int soff) { return __builtin_amdgcn_raw_buffer_load_b128(r, voff, soff, AUX); }
template <int CTRL> __device__ __forceinline__ float dpp(float x) {
  return __builtin_bit_cast(float, __builtin_amdgcn_mov_dpp(__builtin_bit_cast(int, x), CTRL, 0xf, 0xf, true));
}
__device__ __forceinline__ unsigned cvtpk(float lo, float hi) { unsigned r; asm("v_cvt_pk_bf16_f32 %0, %1, %2" : "=v"(r) : "v"(lo), "v"(hi)); return r; }
__device__ __forceinline__ float rbf1(float x) { return __uint_as_float(cvtpk(x, x) << 16); }
__device__ __forceinline__ float xrow_max(float x) {
  auto s = __builtin_amdgcn_permlane16_swap(__float_as_uint(x), __float_as_uint(x), false, false);
  x = fmaxf(__uint_as_float(s[0]), __uint_as_float(s[1]));
  auto t = __builtin_amdgcn_permlane32_swap(__float_as_uint(x), __float_as_uint(x), false, false);
  return fmaxf(__uint_as_float(t[0]), __uint_as_float(t[1]));
}
__device__ __forceinline__ float xrow_sum(float x) {
  auto s = __builtin_amdgcn_permlane16_swap(__float_as_uint(x), __float_as_uint(x), false, false);
  x = __uint_as_float(s[0]) + __uint_as_float(s[1]);
  auto t = __builtin_amdgcn_permlane32_swap(__float_as_uint(x), __float_as_uint(x), false, false);
  return __uint_as_float(t[0]) + __uint_as_float(t[1]);
}
__global__ void __launch_bounds__(512) k2(const float* q, const u16* K, const u16* V, float* pml, float* po) {
  __shared__ float red[8 * 132];
  const int blk = blockIdx.x, tid = threadIdx.x, lane = tid & 63, wave = __builtin_amdgcn_readfirstlane(tid >> 6), r = lane >> 4, c = lane & 15;
  const int bh = blk >> 2;
  rsrc_t rk = mk((const char*)K + (size_t)blk * 262144, 262144);
  rsrc_t rv = mk((const char*)V + (size_t)blk * 262144, 262144);
  const int vkv = (wave >> 2) * 131072 + (wave & 3) * 1024 + lane * 16;
  u32x4 buf[WIN];
  const float4 q0 = *(const float4*)(q + bh * 128 + c * 8), q1 = *(const float4*)(q + bh * 128 + c * 8 + 4);
#pragma unroll
  for (int i = 0; i < WIN; ++i) buf[i] = bl<AUXKV>(rk, vkv, i * 4096);
  unsigned qp[4] = {cvtpk(q0.x, q0.y), cvtpk(q0.z, q0.w), cvtpk(q1.x, q1.y), cvtpk(q1.z, q1.w)};
  float sc[32];
  float m = -INFINITY;
#pragma unroll
  for (int i = 0; i < 32; ++i) {
    u32x4 kv = buf[i % WIN];
    float d = dot2(kv[0], qp[0], 0.f); d = dot2(kv[1], qp[1], d); d = dot2(kv[2], qp[2], d); d = dot2(kv[3], qp[3], d);
    buf[i % WIN] = (i + WIN < 32) ? bl<AUXKV>(rk, vkv, (i + WIN) * 4096) : bl<AUXKV>(rv, vkv, (i + WIN - 32) * 4096);
    d += dpp<0x128>(d); d += dpp<0x124>(d); d += dpp<0x4E>(d); d += dpp<0xB1>(d);
    d = rbf1(rbf1(d) * 0.08838834764831845f);
    sc[i] = d; m = fmaxf(m, d);
    __builtin_amdgcn_sched_barrier(0);
  }
  m = xrow_max(m);
  const float L2E = 1.4426950408889634f;
  float l = 0.f; const float mb = m * L2E;
#pragma unroll
  for (int i = 0; i < 32; ++i) { sc[i] = __builtin_amdgcn_exp2f(sc[i] * L2E - mb); l += sc[i]; }
  float acc[8] = {0, 0, 0, 0, 0, 0, 0, 0};
#pragma unroll
  for (int i = 0; i < 32; ++i) {
    u32x4 vv = buf[i % WIN];
    const float p = sc[i];
#pragma unroll
    for (int k = 0; k < 4; ++k) { acc[2 * k] = fmaf(p, lo16(vv[k]), acc[2 * k]); acc[2 * k + 1] = fmaf(p, hi16(vv[k]), acc[2 * k + 1]); }
    if (i + WIN < 32) buf[i % WIN] = bl<AUXKV>(rv, vkv, (i + WIN) * 4096);
    __builtin_amdgcn_sched_barrier(0);
  }
  l = xrow_sum(l);
#pragma unroll
  for (int k = 0; k < 8; ++k) acc[k] = xrow_sum(acc[k]);
  if (r == 0) { for (int k = 0; k < 8; ++k) red[wave * 132 + 4 + c * 8 + k] = acc[k]; if (c == 0) { red[wave * 132] = m; red[wave * 132 + 1] = l; } }
  __syncthreads();
  if (tid < 128) {
    float M = -INFINITY;
    for (int w = 0; w < 8; ++w) M = fmaxf(M, red[w * 132]);
    float L = 0.f, o = 0.f;
    for (int w = 0; w < 8; ++w) { float e = __builtin_amdgcn_exp2f((red[w * 132] - M) * L2E); L += e * red[w * 132 + 1]; o += e * red[w * 132 + 4 + tid]; }
    po[blk * 128 + tid] = o;
    if (tid == 0) { pml[blk * 2] = M; pml[blk * 2 + 1] = L; }
  }
}

__global__ void __launch_bounds__(256) k3(const u16* x, const float* pml, const float* po, const float* hid, const u16* wo, const u16* wd, __hip_bfloat16* out) {
  __shared__ float o[2 * 4096]; __shared__ float hs[2 * F]; __shared__ float wgt[64 * NSPLIT]; __shared__ float red[4 * 32];
  int tid = threadIdx.x;
  if (tid < 64) { float M = -INFINITY; for (int s = 0; s < NSPLIT; ++s) M = fmaxf(M, pml[(tid * NSPLIT + s) * 2]);
    float L = 0; for (int s = 0; s < NSPLIT; ++s) L += __expf(pml[(tid * NSPLIT + s) * 2] - M) * pml[(tid * NSPLIT + s) * 2 + 1];
    for (int s = 0; s < NSPLIT; ++s) wgt[tid * NSPLIT + s] = __expf(pml[(tid * NSPLIT + s) * 2] - M) / L; }
  for (int i = tid; i < 2 * F; i += 256) hs[i] = hid[i];
  __syncthreads();
  for (int i = tid; i < 8192; i += 256) { int bh = i >> 7, d = i & 127; float a = 0; for (int s = 0; s < NSPLIT; ++s) a += wgt[bh * NSPLIT + s] * po[(bh * NSPLIT + s) * 128 + d]; o[i] = rbf(a); }
  __syncthreads();
  int col0 = blockIdx.x * 8;
  float a0[8] = {0}, a1[8] = {0}, f0[8] = {0}, f1[8] = {0};
  for (int row = tid; row < 4096; row += 256) {
    u32x4 w = *(const u32x4*)(wo + (size_t)row * D + col0);
    float x0 = o[row], x1 = o[4096 + row];
    for (int k = 0; k < 4; ++k) { a0[2*k] += x0 * lo16(w[k]); a0[2*k+1] += x0 * hi16(w[k]); a1[2*k] += x1 * lo16(w[k]); a1[2*k+1] += x1 * hi16(w[k]); }
  }
  for (int row = tid; row < F; row += 256) {
    u32x4 w = *(const u32x4*)(wd + (size_t)row * D + col0);
    float x0 = hs[row], x1 = hs[F + row];
    for (int k = 0; k < 4; ++k) { f0[2*k] += x0 * lo16(w[k]); f0[2*k+1] += x0 * hi16(w[k]); f1[2*k] += x1 * lo16(w[k]); f1[2*k+1] += x1 * hi16(w[k]); }
  }
  int lane = tid & 63, w = tid >> 6;
  for (int k = 0; k < 8; ++k) for (int of = 1; of < 64; of <<= 1) { a0[k] += __shfl_xor(a0[k], of); a1[k] += __shfl_xor(a1[k], of); f0[k] += __shfl_xor(f0[k], of); f1[k] += __shfl_xor(f1[k], of); }
  if (lane == 0) for (int k = 0; k < 8; ++k) { red[w * 32 + k] = a0[k]; red[w * 32 + 8 + k] = a1[k]; red[w * 32 + 16 + k] = f0[k]; red[w * 32 + 24 + k] = f1[k]; }
  __syncthreads();
  if (tid < 16) { int b = tid >> 3, k = tid & 7; float a = 0, f = 0; for (int w2 = 0; w2 < 4; ++w2) { a += red[w2 * 32 + b * 8 + k]; f += red[w2 * 32 + 16 + b * 8 + k]; }
    float xv = bf2f(x[b * D + col0 + k]);
    out[b * D + col0 + k] = __float2bfloat16(rbf(xv + rbf(a)) + rbf(f)); }
}

extern "C" void kernel_launch(void* const* d_in, const int* in_sizes, int n_in, void* d_out, int out_size, void* d_ws, size_t ws_size, hipStream_t stream) {
  const u16* x = (const u16*)d_in[0]; const u16* K = (const u16*)d_in[1]; const u16* V = (const u16*)d_in[2];
  const u16* wq = (const u16*)d_in[3]; const u16* wo = (const u16*)d_in[4];
  const u16* wg = (const u16*)d_in[6]; const u16* wu = (const u16*)d_in[7]; const u16* wd = (const u16*)d_in[8];
  float* ws = (float*)d_ws;
  float* q = ws; float* hid = ws + 8192; float* pml = ws + 16384; float* po = ws + 32768;
  k1<<<64 + 27, 256, 0, stream>>>(x, wq, wg, wu, q, hid);
  k2<<<256, 512, 0, stream>>>(q, K, V, pml, po);
  k3<<<80, 256, 0, stream>>>(x, pml, po, hid, wo, wd, (__hip_bfloat16*)d_out);
}
```

```cpp
#include <hip/hip_runtime.h>
#include <hip/hip_bf16.h>
#include <stdint.h>
#include <utility>

typedef unsigned u32x4 __attribute__((ext_vector_type(4)));
typedef unsigned u32x2 __attribute__((ext_vector_type(2)));
typedef float f32x4 __attribute__((ext_vector_type(4)));
typedef float f32x2 __attribute__((ext_vector_type(2)));
typedef __bf16 bf16x2 __attribute__((ext_vector_type(2)));
typedef __bf16 bf16x8 __attribute__((ext_vector_type(8)));
typedef __amdgpu_buffer_rsrc_t rsrc_t;
typedef __attribute__((address_space(3))) volatile unsigned lds_vu32;

constexpr int CPF = 23500;
constexpr int RING = 20;
constexpr int DQ = 55;
constexpr int DT = 278;
constexpr int DT2 = 278;
constexpr int TOFF = 240;
constexpr int TB0 = 160;
constexpr int PACE = 8;
constexpr int CATCH = 4;
constexpr int BAR0 = 3;
constexpr int NFREE = 3;
constexpr int PAT = 5;
constexpr int DF = 120;
constexpr int DM = 50;
constexpr int D1 = 14;
constexpr int S1 = 4;
constexpr int D2 = 8;
constexpr int S2 = 2;
constexpr int WDT = 185;
constexpr int SPIN_MAX = 200000;
constexpr int QN = 8;

constexpr unsigned WS_QP = 4096;
constexpr unsigned WS_FP = WS_QP + 32 * 16384;
constexpr unsigned WS_HP = WS_FP + 32 * 16384;
constexpr unsigned WS_ML = WS_HP + 32 * 1024;
constexpr unsigned WS_OP = WS_ML + 32 * 1024;
constexpr unsigned WS_YP = WS_OP + 256 * 1024;
constexpr unsigned WS_TB = WS_YP + 160 * 2048;
constexpr unsigned WS_END = WS_TB + 32 * 128;
constexpr unsigned WS_DBG = 4u << 20;

constexpr float L2E = 1.4426950408889634f;

__device__ __forceinline__ rsrc_t mk(const void* p, unsigned bytes) { return __builtin_amdgcn_make_buffer_rsrc((void*)p, 0, bytes, 0x00020000); }
template <int AUX> __device__ __forceinline__ u32x4 bl4(rsrc_t r, int voff, int soff) { return __builtin_amdgcn_raw_buffer_load_b128(r, voff, soff, AUX); }
template <int AUX> __device__ __forceinline__ u32x2 bl2(rsrc_t r, int voff, int soff) { return __builtin_amdgcn_raw_buffer_load_b64(r, voff, soff, AUX); }
template <int AUX> __device__ __forceinline__ unsigned bl1(rsrc_t r, int voff, int soff) { return __builtin_amdgcn_raw_buffer_load_b32(r, voff, soff, AUX); }
__device__ __forceinline__ void st4(u32x4 v, rsrc_t r, int voff, int soff) { __builtin_amdgcn_raw_buffer_store_b128(v, r, voff, soff, 16); }
__device__ __forceinline__ void st2(u32x2 v, rsrc_t r, int voff, int soff) { __builtin_amdgcn_raw_buffer_store_b64(v, r, voff, soff, 16); }
template <int CTRL> __device__ __forceinline__ float dpp(float x) {
  return __builtin_bit_cast(float, __builtin_amdgcn_mov_dpp(__builtin_bit_cast(int, x), CTRL, 0xf, 0xf, true));
}
template <int CTRL> __device__ __forceinline__ unsigned dppu(unsigned x) { return (unsigned)__builtin_amdgcn_mov_dpp((int)x, CTRL, 0xf, 0xf, true); }
__device__ __forceinline__ unsigned cvtpk(float lo, float hi) { f32x2 v = {lo, hi}; return __builtin_bit_cast(unsigned, __builtin_convertvector(v, bf16x2)); }
__device__ __forceinline__ float rbf(float x) { return __uint_as_float(cvtpk(x, x) << 16); }
__device__ __forceinline__ float lo16(unsigned v) { return __uint_as_float(v << 16); }
__device__ __forceinline__ float hi16(unsigned v) { return __uint_as_float(v & 0xffff0000u); }
__device__ __forceinline__ float f(unsigned v) { return __uint_as_float(v); }
__device__ __forceinline__ unsigned u(float v) { return __float_as_uint(v); }
__device__ __forceinline__ float dot2(unsigned k, unsigned q, float acc) {
  return __builtin_amdgcn_fdot2_f32_bf16(__builtin_bit_cast(bf16x2, k), __builtin_bit_cast(bf16x2, q), acc, false);
}
__device__ __forceinline__ float xrow_max(float x) {
  auto s = __builtin_amdgcn_permlane16_swap(u(x), u(x), false, false);
  x = fmaxf(f(s[0]), f(s[1]));
  auto t = __builtin_amdgcn_permlane32_swap(u(x), u(x), false, false);
  return fmaxf(f(t[0]), f(t[1]));
}
__device__ __forceinline__ float xrow_sum(float x) {
  auto s = __builtin_amdgcn_permlane16_swap(u(x), u(x), false, false);
  x = f(s[0]) + f(s[1]);
  auto t = __builtin_amdgcn_permlane32_swap(u(x), u(x), false, false);
  return f(t[0]) + f(t[1]);
}
__device__ __forceinline__ f32x4 rowmma(unsigned pp, const u32x4& msk, u32x4 w, f32x4 acc) {
  u32x4 a = {pp & msk[0], pp & msk[1], pp & msk[2], pp & msk[3]};
  return __builtin_amdgcn_mfma_f32_16x16x32_bf16(__builtin_bit_cast(bf16x8, a), __builtin_bit_cast(bf16x8, w), acc, 0, 0, 0);
}
__device__ __forceinline__ void wg_barrier() {
  __builtin_amdgcn_sched_barrier(0);
  asm volatile("s_waitcnt lgkmcnt(0)\n\ts_barrier" ::: "memory");
  __builtin_amdgcn_sched_barrier(0);
}
__device__ __forceinline__ unsigned bad4(const u32x4& v, unsigned tag) { return (v[1] ^ tag) | (v[3] ^ tag); }
__device__ __forceinline__ void cfence() { asm volatile("" ::: "memory"); }
template <int N> __device__ __forceinline__ void vwait() { asm volatile("s_waitcnt vmcnt(%0)" ::"n"(N) : "memory"); }

template <typename F, int... I> __device__ __forceinline__ void sfor_impl(F&& fn, std::integer_sequence<int, I...>) { (fn(std::integral_constant<int, I>{}), ...); }
template <int N, typename F> __device__ __forceinline__ void sfor(F&& fn) { sfor_impl(fn, std::make_integer_sequence<int, N>{}); }

struct Sched { int pre[65]; int post[65]; };
constexpr Sched make_sched() {
  Sched s{}; int cp = 0;
  for (int t = 0; t < 65; ++t) { s.pre[t] = 0; s.post[t] = 0; }
  for (int t = RING; t < 64; ++t) {
    int c1 = cp > t - RING + 1 ? cp : t - RING + 1;
    int lim = t - PACE + 1;
    int c2 = cp + CATCH < lim ? cp + CATCH : lim;
    if (c2 < c1) c2 = c1;
    s.pre[t] = c1; s.post[t] = c2; cp = c2;
  }
  return s;
}
constexpr Sched SCH = make_sched();

__device__ __forceinline__ void load40(rsrc_t rx, int off, unsigned (&d)[10]) {
  const u32x4 a = bl4<0>(rx, off, 0), b = bl4<0>(rx, off + 16, 0); const u32x2 c = bl2<0>(rx, off + 32, 0);
  d[0] = a[0]; d[1] = a[1]; d[2] = a[2]; d[3] = a[3]; d[4] = b[0]; d[5] = b[1]; d[6] = b[2]; d[7] = b[3]; d[8] = c[0]; d[9] = c[1];
}
__device__ __forceinline__ void load_x(rsrc_t rx, int lane, int j, unsigned (&sd)[10], unsigned (&rd)[10]) {
  const int g = lane >> 4, bb = (lane >> 3) & 1, jj = lane & 7;
  load40(rx, bb * 1280 + (g * 8 + jj) * 40, sd);
  load40(rx, bb * 1280 + j * 160 + g * 40, rd);
}
__device__ __forceinline__ float sum_batch(float v) {
  v += dpp<0xB1>(v); v += dpp<0x4E>(v); v += dpp<0x141>(v);
  return xrow_sum(v);
}
__device__ __forceinline__ void layernorm_rows(const unsigned (&sd)[10], const unsigned (&rd)[10], unsigned (&xa)[20]) {
  float s = 0.f;
#pragma unroll
  for (int i = 0; i < 10; ++i) s += lo16(sd[i]) + hi16(sd[i]);
  const float mean = sum_batch(s) * (1.f / 640.f);
  float q = 0.f;
#pragma unroll
  for (int i = 0; i < 10; ++i) { float a = lo16(sd[i]) - mean, b = hi16(sd[i]) - mean; q = fmaf(a, a, q); q = fmaf(b, b, q); }
  const float var = sum_batch(q) * (1.f / 640.f);
  const float mb = rbf(mean);
  const float den = rbf(__builtin_sqrtf(rbf(rbf(var) + 1e-5f)));
  const float inv = 1.f / den;
#pragma unroll
  for (int i = 0; i < 10; ++i) {
    unsigned d = cvtpk(lo16(rd[i]) - mb, hi16(rd[i]) - mb);
    float a = lo16(d) * inv, b = hi16(d) * inv;
    xa[2 * i] = cvtpk(a, a); xa[2 * i + 1] = cvtpk(b, b);
  }
}


__device__ __forceinline__ void pace(unsigned& nd, unsigned inc) {
  asm volatile("1:\n\ts_memrealtime vcc\n\ts_waitcnt lgkmcnt(0)\n\ts_lshl4_add_u32 vcc_lo, vcc_lo, %0\n\ts_cmp_lt_i32 vcc_lo, 0\n\ts_cbranch_scc1 1b\n\ts_sub_u32 %0, %0, %1"
               : "+s"(nd) : "s"(inc) : "vcc", "scc", "memory");
}
__device__ __forceinline__ unsigned origin(unsigned t0) {
  const int d = (int)((unsigned)__builtin_amdgcn_s_memrealtime() - t0) - TB0;
  return t0 + (d > 0 ? d : 0);
}
template <int N> __device__ __forceinline__ void delay() {
  if constexpr (N > 127) { __builtin_amdgcn_s_sleep(127); delay<N - 127>(); }
  else if constexpr (N > 0) __builtin_amdgcn_s_sleep(N);
}

__global__ void __launch_bounds__(640, 3) fused_decode(const char* __restrict__ x, const char* __restrict__ K, const char* __restrict__ V,
                                                       const char* __restrict__ wq, const char* __restrict__ wo, const char* __restrict__ wg,
                                                       const char* __restrict__ wu, const char* __restrict__ wd, char* __restrict__ out, char* ws) {
  __shared__ __attribute__((aligned(16))) unsigned s_q[64];
  __shared__ __attribute__((aligned(16))) unsigned s_flag[4];
  __shared__ __attribute__((aligned(16))) unsigned s_ml[16];
  __shared__ __attribute__((aligned(16))) unsigned s_h[64];
  __shared__ __attribute__((aligned(16))) float s_o[8 * 128];
  __shared__ __attribute__((aligned(16))) float s_y[8 * 160];

  const int blk = blockIdx.x, lane = threadIdx.x & 63, wave = __builtin_amdgcn_readfirstlane(threadIdx.x >> 6);
  const int h = (blk & 7) * 4 + (blk >> 6), j = (blk >> 3) & 7;
  const int g = lane >> 4, c = lane & 15;
  const int hf = blk < 192 ? (blk & 7) * 3 + (blk >> 6) : 24 + ((blk - j) & 7);
  const int hr = blk < 128 ? (blk & 7) * 2 + (blk >> 6) : blk < 192 ? 16 + ((blk - j) & 7) : 32;
  const unsigned t0 = (unsigned)__builtin_amdgcn_s_memrealtime();
  u32x4 cpf = {0u, 0u, 0u, 0u};
  if (wave == 1) cpf = bl4<0>
(mk((const void*)(__builtin_amdgcn_s_getpc() & ~255ull), CPF), (blk >> 3) * 1024 + lane * 16, 0);
  asm volatile("" :: "s"(ws), "s"(x), "s"(K), "s"(V), "s"(wq), "s"(wo), "s"(wg), "s"(wu), "s"(wd), "s"(out));
  const rsrc_t rws = mk(ws, WS_END);
  u32x4 msk;
  {
    const int jj = lane & 7; const unsigned hm = (jj & 1) ? 0xffff0000u : 0x0000ffffu;
#pragma unroll
    for (int w = 0; w < 4; ++w) msk[w] = ((jj >> 1) == w) ? hm : 0u;
  }
  const f32x4 zero4 = {0.f, 0.f, 0.f, 0.f};

  auto publish_y = [&](int part, unsigned tag) {
    if (lane < 40) {
      const int k = 10 * part + (lane >> 2), o = ((lane >> 1) & 1) * 80 + 4 * k + 2 * (lane & 1);
      f32x2 a = *(const f32x2*)&s_y[o];
#pragma unroll
      for (int w = 1; w < 8; ++w) a += *(const f32x2*)&s_y[w * 160 + o];
      u32x4 v = {u(a[0]), tag, u(a[1]), tag};
      st4(v, rws, ((j * 20 + k) * 32 + h) * 64 + (lane & 3) * 16, WS_YP);
    }
  };

  if (wave == 0) {
    __builtin_amdgcn_s_setprio(3);
    const unsigned E = bl1<16>(rws, 0, 0);
    unsigned sd[10], rd[10];
    load_x(mk(x, 2560), lane, j, sd, rd);
    const rsrc_t rq = mk(wq + (size_t)j * (80 * 8192) + h * 256, 80 * 8192);
    u32x4 w[20];
#pragma unroll
    for (int i = 0; i < 20; ++i) { w[i] = bl4<2>(rq, g * (20 * 8192) + c * 16, i * 8192); __builtin_amdgcn_sched_barrier(0); }
    if (lane == 0) s_flag[0] = 0;
    if (lane < 16) s_ml[lane] = 0;
    wg_barrier();
    const unsigned tz = origin(t0);
    unsigned xa[20];
    layernorm_rows(sd, rd, xa);
    const unsigned tag = __builtin_amdgcn_readfirstlane(E) * 1664525u + 1013904223u;
    f32x4 acc = zero4;
#pragma unroll
    for (int i = 0; i < 20; ++i) acc = rowmma(xa[i], msk, w[i], acc);
    {
      const int o = h * 16384 + (g >> 1) * 8192 + j * 1024 + ((g & 1) * 16 + c) * 16;
      u32x4 v0 = {u(acc[0]), tag, u(acc[1]), tag}, v1 = {u(acc[2]), tag, u(acc[3]), tag};
      st4(v0, rws, o, WS_QP); st4(v1, rws, o + 512, WS_QP);
      if (blk < 8 && lane < 32) { u32x2 tb = {tz, tag}; st2(tb, rws, lane * 128 + blk * 8, WS_TB); }
    }
    delay<DQ>();
    float q0, q1;
    const u32x2 tb = bl2<16>(rws, h * 128 + (lane & 7) * 8, WS_TB);
    int spin = 0;
    for (;; ++spin) {
      cfence();
      u32x4 v[8];
#pragma unroll
      for (int p = 0; p < QN; ++p) v[p] = bl4<16>(rws, h * 16384 + (j >> 2) * 8192 + lane * 16 + p * 1024, WS_QP);
      unsigned bad = 0; q0 = 0.f; q1 = 0.f;
#pragma unroll
      for (int p = 0; p < QN; ++p) { bad |= bad4(v[p], tag); q0 += f(v[p][0]); q1 += f(v[p][2]); }
      if (__all(bad == 0) || spin >= SPIN_MAX) break;
      __builtin_amdgcn_s_sleep(16);
    }
    s_q[4 * c + 2 * (g & 1) + (g >> 1)] = cvtpk(q0, q1);
    if (lane == 0) s_flag[1] = tag;
    {
      int d = tb[1] == tag ? (int)(tb[0] - tz) : 0;
      d = d < -300 ? -300 : (d > 300 ? 300 : d);
      d += (int)dppu<0xB1>(d); d += (int)dppu<0x4E>(d); d += (int)dppu<0x141>(d);
      if (lane == 0) s_flag[2] = tz + (d >> 3);
    }
    cfence();
    if (lane == 0) *(lds_vu32*)&s_flag[0] = 1;
    unsigned mw, lw;
    for (;;) {
      lw = *(lds_vu32*)&s_ml[2 * (lane & 7) + 1];
      if (__all(lw != 0)) break;
      __builtin_amdgcn_s_sleep(4);
    }
    mw = *(lds_vu32*)&s_ml[2 * (lane & 7)];
    float Mb = fmaxf(f(mw), dpp<0xB1>(f(mw))); Mb = fmaxf(Mb, dpp<0x4E>(Mb)); Mb = fmaxf(Mb, dpp<0x141>(Mb));
    float Lb = __builtin_amdgcn_exp2f((f(mw) - Mb) * L2E) * f(lw);
    Lb += dpp<0xB1>(Lb); Lb += dpp<0x4E>(Lb); Lb += dpp<0x141>(Lb);
    if (lane == 0) { u32x4 ml = {u(Mb), tag, u(Lb), tag}; st4(ml, rws, (h * 8 + j) * 128, WS_ML); }
    delay<DM>();
    u32x4 ml = {0u, tag, 0u, tag};
    bool need = lane < 4;
    for (spin = 0;; ++spin) {
      cfence();
      if (need) ml = bl4<16>(rws, (h * 8 + (j & 4) + lane) * 128, WS_ML);
      need = bad4(ml, tag) != 0;
      if (!__any(need) || spin >= SPIN_MAX) break;
      __builtin_amdgcn_s_sleep(16);
    }
    float M = fmaxf(f(ml[0]), dpp<0xB1>(f(ml[0]))); M = fmaxf(M, dpp<0x4E>(M));
    float L = __builtin_amdgcn_exp2f((f(ml[0]) - M) * L2E) * f(ml[2]);
    L += dpp<0xB1>(L); L += dpp<0x4E>(L);
    M = f(__builtin_amdgcn_readfirstlane(u(M))); L = f(__builtin_amdgcn_readfirstlane(u(L)));
    const float fw = __builtin_amdgcn_exp2f((f(mw) - M) * L2E) / L;
    float fs[8];
#pragma unroll
    for (int w2 = 0; w2 < 8; ++w2) fs[w2] = f(__builtin_amdgcn_readlane(u(fw), w2));
    wg_barrier();
    {
      f32x2 ov[8];
#pragma unroll
      for (int w2 = 0; w2 < 8; ++w2) ov[w2] = *(const f32x2*)&s_o[w2 * 128 + 2 * lane];
      float a0 = 0.f, a1 = 0.f;
#pragma unroll
      for (int w2 = 0; w2 < 8; ++w2) { a0 = fmaf(fs[w2], ov[w2][0], a0); a1 = fmaf(fs[w2], ov[w2][1], a1); }
      u32x4 v = {u(a0), tag, u(a1), tag};
      st4(v, rws, (h * 8 + j) * 1024 + lane * 16, WS_OP);
    }
    wg_barrier();
    publish_y(0, tag);
  } else if (wave == 1) {
    __builtin_amdgcn_s_setprio(2);
    const unsigned E = bl1<16>(rws, 0, 0);
    const rsrc_t rx = mk(x, 2560);
    unsigned sd[10], rd[10];
    load_x(rx, lane, j, sd, rd);
    const int ocol = ((lane >> 1) & 1) * 640 + 80 * j + 4 * hr + 2 * (lane & 1);
    const unsigned xres = bl1<0>(rx, ocol * 2, 0);
    unsigned pt0, pt1 = 0;
    {
      const int bi = h * 8 + j;
      int po = lane < 16 ? WS_QP + bi * 2048 + lane * 128
             : lane < 32 ? (hf < 27 ? WS_FP + (hf * 8 + j) * 2048 + (lane - 16) * 128 : 0x7ff00000)
             : lane < 48 ? (hr < 20 ? WS_YP + (j * 20 + hr) * 2048 + (lane - 32) * 128 : 0x7ff00000)
             : lane < 56 ? WS_OP + bi * 1024 + (lane - 48) * 128
             : lane == 56 ? WS_HP + (hf * 8 + j) * 128 : lane == 57 ? WS_ML + bi * 128 : lane == 58 && j == 0 ? WS_TB + h * 128 : 0x7ff00000;
      pt0 = bl1<16>(rws, po, 0);
      pt1 = bl1<16>(rws, po, 64);
      {
        pt1 ^= bl1<2>(mk(V + (((size_t)((j >> 2) * 32 + h)) << 20), 4), lane == 0 ? 0 : 0x7ff00000, 0);
        pt1 ^= bl1<0>(mk(wo + (size_t)h * (128 * 1280), 128 * 1280), lane < 2 ? lane * 127 * 1280 + j * 160 : 0x7ff00000, 0);
        pt1 ^= bl1<0>(mk(wd + (size_t)hf * (64 * 1280), hf < 27 ? 64 * 1280 : 0), lane < 2 ? lane * 63 * 1280 + j * 160 : 0x7ff00000, 0);
      }
      pt1 ^= bl1<0>(mk(wg, 640 * 3456), lane < 2 && hf < 27 ? (80 * j + 79 * lane) * 3456 + hf * 128 : 0x7ff00000, 0);
      pt1 ^= bl1<0>(mk(wu, 640 * 3456), lane < 2 && hf < 27 ? (80 * j + 79 * lane) * 3456 + hf * 128 : 0x7ff00000, 0);
      pt1 ^= bl1<16>(mk(out, 2560), blk == 0 ? lane * 64 : 0x7ff00000, 0);
    }
    wg_barrier();
    unsigned tag = 0;
    unsigned hval = 0;
    if (hf < 27) {
      const char* p = (c < 8 ? wg : wu) + (size_t)((80 * j + 20 * g) * 3456 + hf * 128 + (c & 7) * 16);
      u32x4 w[20];
#pragma unroll
      for (int i = 0; i < 20; ++i) {
        w[i] = __builtin_nontemporal_load((const u32x4*)(p + i * 3456));
        __builtin_amdgcn_sched_barrier(0);
      }
      __builtin_amdgcn_sched_barrier(0);
      unsigned xa[20];
      layernorm_rows(sd, rd, xa);
      tag = __builtin_amdgcn_readfirstlane(E) * 1664525u + 1013904223u;
      f32x4 acc = zero4;
#pragma unroll
      for (int i = 0; i < 20; ++i) acc = rowmma(xa[i], msk, w[i], acc);
      {
        const int o = hf * 16384 + (c & 7) * 2048 + j * 256 + ((c >> 3) * 4 + g) * 16;
        u32x4 v0 = {u(acc[0]), tag, u(acc[1]), tag}, v1 = {u(acc[2]), tag, u(acc[3]), tag};
        st4(v0, rws, o, WS_FP); st4(v1, rws, o + 128, WS_FP);
      }
      delay<DF>();
      float a0, a1;
      int spin = 0;
      for (;; ++spin) {
        cfence();
        const u32x4 v0 = bl4<16>(rws, hf * 16384 + j * 2048 + lane * 16, WS_FP), v1 = bl4<16>(rws, hf * 16384 + j * 2048 + lane * 16 + 1024, WS_FP);
        const bool ok = (bad4(v0, tag) | bad4(v1, tag)) == 0;
        a0 = f(v0[0]) + f(v1[0]); a1 = f(v0[2]) + f(v1[2]);
        if (__all(ok) || spin >= SPIN_MAX) break;
        __builtin_amdgcn_s_sleep(32);
      }
      a0 = rbf(xrow_sum(a0)); a1 = rbf(xrow_sum(a1));
      const float u0 = dpp<0x104>(a0), u1 = dpp<0x104>(a1);
      auto gelu = [](float v) {
        const float z = 0.7978845608028654f * (v + 0.044715f * v * v * v);
        const float t = 1.f - 2.f / (1.f + __builtin_amdgcn_exp2f(2.f * L2E * z));
        return 0.5f * v * (1.f + t);
      };
      const unsigned hp = cvtpk(rbf(gelu(a0)) * u0, rbf(gelu(a1)) * u1);
      if (lane < 16 && (c & 4) == 0) {
        const int gp = c & 3, st = c >> 3;
        u32x2 v = {hp, tag};
        st2(v, rws, (hf * 8 + j) * 128 + ((gp >> 1) * 4 + 2 * (gp & 1) + st) * 8, WS_HP);
      }
      delay<DF>();
      for (;; ++spin) {
        cfence();
        const u32x2 hv = bl2<16>(rws, (hf * 8 + (lane >> 3)) * 128 + (lane & 7) * 8, WS_HP);
        hval = hv[0];
        if (__all(hv[1] == tag) || spin >= SPIN_MAX) break;
        __builtin_amdgcn_s_sleep(32);
      }
    }
    asm volatile("" :: "v"(cpf));
    asm volatile("" :: "v"(pt0), "v"(pt1));
    s_h[lane] = hval;
    wg_barrier();
    wg_barrier();
    if (hr < 20) {
      delay<D2>();
      u32x4 v0 = bl4<16>(rws, (j * 20 + hr) * 2048 + lane * 16, WS_YP), v1 = bl4<16>(rws, (j * 20 + hr) * 2048 + lane * 16 + 1024, WS_YP);
      int spin = 0;
      for (;; ++spin) {
        const bool n0 = bad4(v0, tag) != 0, n1 = bad4(v1, tag) != 0;
        if (!__any(n0 || n1) || spin >= SPIN_MAX) break;
        delay<S2>();
        cfence();
        const u32x4 t0 = bl4<16>(rws, n0 ? (j * 20 + hr) * 2048 + lane * 16 : 0x7ff00000, WS_YP);
        const u32x4 t1 = bl4<16>(rws, n1 ? (j * 20 + hr) * 2048 + lane * 16 + 1024 : 0x7ff00000, WS_YP);
        v0 = n0 ? t0 : v0; v1 = n1 ? t1 : v1;
      }
      float a0 = f(v0[0]) + f(v1[0]), a1 = f(v0[2]) + f(v1[2]);
      a0 += dpp<0x124>(a0); a1 += dpp<0x124>(a1);
      a0 += dpp<0x128>(a0); a1 += dpp<0x128>(a1);
      a0 = xrow_sum(a0); a1 = xrow_sum(a1);
      if (lane < 4) __builtin_amdgcn_raw_buffer_store_b32(cvtpk(lo16(xres) + a0, hi16(xres) + a1), mk(out, 2560), ocol * 2, 0, 0);
      if (blk == 0 && lane == 0) __builtin_amdgcn_raw_buffer_store_b32(tag, rws, 0, 0, 16);
    }
  } else {
    const int sw = wave - 2;
    const size_t kvbase = ((size_t)((j >> 2) * 32 + h)) << 20;
    const rsrc_t rk = mk(K + kvbase, 1 << 20), rv = mk(V + kvbase, 1 << 20);
    const int wi = (j & 3) * 8 + sw;
    constexpr int KSTEP = 1024 << PAT;
    const int vb = (wi >> PAT) * (32768 << PAT) + (wi & ((1 << PAT) - 1)) * 1024;
    const int vv = vb + lane * 16;
    const int vk = vb + (c >> 2) * 256 + g * 64 + (c & 3) * 16;
    const rsrc_t ro = mk(wo + (size_t)h * (128 * 1280) + j * 160, 128 * 1280 - j * 160);
    const rsrc_t rd = mk(wd + (size_t)hf * (64 * 1280) + j * 160, hf < 27 ? 64 * 1280 - j * 160 : 0);
    const int vo = c < 10 ? (16 * sw + 4 * g) * 1280 + c * 16 : 0x7ff00000;
    const int vd = c < 10 ? (8 * sw + 2 * g) * 1280 + c * 16 : 0x7ff00000;
    u32x4 buf[64];
    unsigned inc = DT * 16 / 10, nd = 0, tz = 0;
    auto issue = [&](auto T) { constexpr int t = T;
      constexpr int so = (t & 31) * KSTEP;
      buf[t] = t < 32 ? bl4<2>(rk, vk, so) : bl4<2>(rv, vv, so); };
    sfor<RING>([&](auto T) {
      constexpr int t = T;
      if constexpr (t == BAR0) { wg_barrier(); tz = origin(t0); nd = 0u - (tz * 16u + (TOFF * 10 + NFREE * DT) * 16 / 10); }
      if constexpr (t >= PACE) vwait<PACE - 1>();
      if constexpr (t >= NFREE) pace(nd, inc);
      issue(T);
      __builtin_amdgcn_sched_barrier(0);
    });
    while (__builtin_amdgcn_readfirstlane(*(lds_vu32*)&s_flag[0]) == 0) __builtin_amdgcn_s_sleep(1);
    cfence();
    u32x4 qb[4];
    {
      const u32x4 qp = *(const u32x4*)&s_q[16 * g + 4 * (c & 3)], z = {0u, 0u, 0u, 0u};
#pragma unroll
      for (int i = 0; i < 4; ++i) qb[i] = (c >> 2) == i ? qp : z;
    }
    const unsigned tag = __builtin_amdgcn_readfirstlane(s_flag[1]);
    inc = DT2 * 16 / 10 + (unsigned)(((int)(__builtin_amdgcn_readfirstlane(s_flag[2]) - tz) * (16384 / (64 - RING))) >> 10);
    float sc[8];
    unsigned pp[8];
    f32x4 acc = zero4, sa = zero4;
    auto consume = [&](auto I) {
      constexpr int i = I;
      if constexpr (i < 32) {
        sa = __builtin_amdgcn_mfma_f32_16x16x32_bf16(__builtin_bit_cast(bf16x8, buf[i]), __builtin_bit_cast(bf16x8, qb[i & 3]), (i & 3) ? sa : zero4, 0, 0, 0);
        if constexpr ((i & 3) == 3) {
          const float d0 = (c & 1) ? sa[1] : sa[0], d1 = (c & 1) ? sa[3] : sa[2];
          float d = (c & 2) ? d1 : d0;
          d += dpp<0xB1>(d); d += dpp<0x4E>(d);
          sc[i >> 2] = rbf(rbf(d) * 0.08838834764831845f);
        }
        if constexpr (i == 31) {
          float m = fmaxf(fmaxf(fmaxf(sc[0], sc[1]), fmaxf(sc[2], sc[3])), fmaxf(fmaxf(sc[4], sc[5]), fmaxf(sc[6], sc[7])));
          m = fmaxf(m, dpp<0x124>(m)); m = fmaxf(m, dpp<0x128>(m));
          m = xrow_max(m);
          const float mb = m * L2E;
          float l = 0.f;
#pragma unroll
          for (int k = 0; k < 8; ++k) { const float p = __builtin_amdgcn_exp2f(fmaf(sc[k], L2E, -mb)); l += p; pp[k] = cvtpk(p, p); }
          l += dpp<0x124>(l); l += dpp<0x128>(l);
          l = xrow_sum(l);
          if (lane == 0) { u32x2 v = {u(m), u(l)}; *(u32x2*)&s_ml[2 * sw] = v; }
        }
      } else {
        acc = rowmma(dppu<0x150 + 4 * (i & 3)>(pp[(i - 32) >> 2]), msk, buf[i], acc);
      }
    };
    u32x4 wv[6];
    auto wload = [&](auto I) { constexpr int i = I; wv[i] = i >= 4 ? bl4<2>(rd, vd, (i - 4) * 1280) : bl4<2>(ro, vo, i * 1280); };
    sfor<64 - RING>([&](auto T0) {
      constexpr int t = T0 + RING;
      constexpr int c0 = t == RING ? 0 : SCH.post[t - 1], c1 = SCH.pre[t], c2 = SCH.post[t];
      sfor<c1 - c0>([&](auto I) { consume(std::integral_constant<int, c0 + I>{}); });
      __builtin_amdgcn_sched_barrier(0);
      vwait<PACE - 1>();
      pace(nd, inc);
      issue(std::integral_constant<int, t>{});
      __builtin_amdgcn_sched_barrier(0);
      sfor<c2 - c1>([&](auto I) { consume(std::integral_constant<int, c1 + I>{}); });
      __builtin_amdgcn_sched_barrier(0);
    });
    constexpr int CE = SCH.post[63];
    sfor<64 - CE>([&](auto I) {
      constexpr int i = I;
      consume(std::integral_constant<int, CE + i>{});
      __builtin_amdgcn_sched_barrier(0);
      if constexpr (CE + i >= 56 && CE + i < 62) { pace(nd, WDT * 16 / 10); wload(std::integral_constant<int, 61 - (CE + i)>{}); }
      __builtin_amdgcn_sched_barrier(0);
    });
    if (lane < 32) *(f32x4*)&s_o[sw * 128 + 8 * c + 4 * g] = acc;
    wg_barrier();
    const int bb = (lane >> 3) & 1, half = (lane >> 2) & 1;
    const int po = (h * 8 + bb * 4 + (lane & 3)) * 1024 + (16 * sw + 4 * g + 2 * half) * 8;
    const unsigned hv = s_h[sw * 8 + bb * 4 + g];
    delay<D1>();
    u32x4 ov = bl4<16>(rws, po, WS_OP);
    __builtin_amdgcn_sched_barrier(0);
    f32x4 y = rowmma(__builtin_amdgcn_perm(hv, hv, 0x01000100), msk, wv[4], zero4);
    y = rowmma(__builtin_amdgcn_perm(hv, hv, 0x03020302), msk, wv[5], y);
    __builtin_amdgcn_sched_barrier(0);
    int spin = 0;
    for (;; ++spin) {
      const bool need = bad4(ov, tag) != 0;
      if (!__any(need) || spin >= SPIN_MAX) break;
      delay<S1>();
      cfence();
      const u32x4 t = bl4<16>(rws, need ? po : 0x7ff00000, WS_OP);
      ov = need ? t : ov;
    }
    {
      float o0 = f(ov[0]), o1 = f(ov[2]);
      o0 += dpp<0xB1>(o0); o1 += dpp<0xB1>(o1);
      o0 += dpp<0x4E>(o0); o1 += dpp<0x4E>(o1);
      const unsigned pk = cvtpk(o0, o1), ot = dppu<0x141>(pk);
      const unsigned lo = half ? ot : pk, hi = half ? pk : ot;
      f32x4 y2 = rowmma(__builtin_amdgcn_perm(lo, lo, 0x01000100), msk, wv[0], zero4);
      y = rowmma(__builtin_amdgcn_perm(lo, lo, 0x03020302), msk, wv[1], y);
      y2 = rowmma(__builtin_amdgcn_perm(hi, hi, 0x01000100), msk, wv[2], y2);
      y = rowmma(__builtin_amdgcn_perm(hi, hi, 0x03020302), msk, wv[3], y);
      y += y2;
      if (c < 10) *(f32x4*)&s_y[sw * 160 + (g >> 1) * 80 + 8 * c + 4 * (g & 1)] = y;
    }
    wg_barrier();
    if (sw == 0) publish_y(1, tag);
  }
}

extern "C" void kernel_launch(void* const* d_in, const int* in_sizes, int n_in, void* d_out, int out_size, void* d_ws, size_t ws_size, hipStream_t stream) {
  fused_decode<<<256, 640, 0, stream>>>((const char*)d_in[0], (const char*)d_in[1], (const char*)d_in[2], (const char*)d_in[3], (const char*)d_in[4],
                                        (const char*)d_in[6], (const char*)d_in[7], (const char*)d_in[8], (char*)d_out, (char*)d_ws);
}
```

```cpp
#include <hip/hip_runtime.h>
#include <hip/hip_bf16.h>
#include <stdint.h>
#include <utility>

typedef unsigned u32x16 __attribute__((ext_vector_type(16)));
typedef unsigned u32x4 __attribute__((ext_vector_type(4)));
typedef unsigned u32x2 __attribute__((ext_vector_type(2)));
typedef float f32x4 __attribute__((ext_vector_type(4)));
typedef float f32x2 __attribute__((ext_vector_type(2)));
typedef __bf16 bf16x2 __attribute__((ext_vector_type(2)));
typedef __bf16 bf16x8 __attribute__((ext_vector_type(8)));
typedef short s16x4 __attribute__((ext_vector_type(4)));
typedef __amdgpu_buffer_rsrc_t rsrc_t;
typedef __attribute__((address_space(1))) const char* gptr;
typedef __attribute__((address_space(3))) volatile unsigned lds_vu32;
typedef __attribute__((address_space(3))) f32x2 lds_f32x2;
typedef __attribute__((address_space(3))) f32x4 lds_f32x4;

constexpr int RING = 20;
constexpr int DQ = 55;
constexpr int NYP = 8;
constexpr int TB0 = 150;
constexpr int D1T = 35;
constexpr int WDT = 220;
constexpr int GG = 114;
constexpr int W3D = 14;
constexpr int CPF = 21504;
constexpr int TRD = 120;
constexpr int TRX = 206;
constexpr int G2 = 8;
constexpr int G3 = 12;
constexpr int NC2 = 3;
constexpr int DT = 282;
constexpr int DT2 = 282;
constexpr int LT = 7;
constexpr int WNT = 12;
constexpr int TOFF = 158;
constexpr int CK = 4;
constexpr int CZ = 10;
constexpr int PACE = 8;
constexpr int CATCH = 4;
constexpr int BAR0 = 3;
constexpr int NFREE = 3;
constexpr int PAT = 5;
constexpr int DF = 120;
constexpr int DM = 50;
constexpr int S1 = 4;
constexpr int DUMP = 16;
constexpr int YST = 128;
constexpr int TA = 1;
constexpr int PWA = 0;
constexpr int HT = 44;
constexpr int OAUX = 0;
constexpr int SPIN_MAX = 200000;
constexpr int SA = 16;
constexpr int QAUX = 2;
constexpr int NOPT = 0;
constexpr int QN = 8;

constexpr unsigned WS_QP = 4096;
constexpr unsigned WS_FP = WS_QP + 32 * 16384;
constexpr unsigned WS_HP = WS_FP + 32 * 16384;
constexpr unsigned WS_ML = WS_HP + 32 * 1024;
constexpr unsigned WS_OP = WS_ML + 32 * 1024;
constexpr unsigned CS = 256 * 1024;
constexpr unsigned WS_YP = WS_OP + CS;
constexpr unsigned WS_TB = WS_YP + 3 * CS;
constexpr unsigned YC1 = NC2 > 1 ? CS : 0, YC2 = NC2 > 2 ? 2 * CS : 0;
constexpr unsigned WS_LG = WS_TB + 32 * 128;
constexpr unsigned WS_END = WS_LG + 16384;

constexpr float L2E = 1.4426950408889634f;

__device__ __forceinline__ rsrc_t mk(const void* p, unsigned bytes) { return __builtin_amdgcn_make_buffer_rsrc((void*)p, 0, bytes, 0x00020000); }
template <int AUX> __device__ __forceinline__ u32x4 bl4(rsrc_t r, int voff, int soff) { return __builtin_amdgcn_raw_buffer_load_b128(r, voff, soff, AUX); }
template <int AUX> __device__ __forceinline__ u32x2 bl2(rsrc_t r, int voff, int soff) { return __builtin_amdgcn_raw_buffer_load_b64(r, voff, soff, AUX); }
template <int AUX> __device__ __forceinline__ unsigned bl1(rsrc_t r, int voff, int soff) { return __builtin_amdgcn_raw_buffer_load_b32(r, voff, soff, AUX); }
__device__ __forceinline__ void st4(u32x4 v, rsrc_t r, int voff, int soff) { __builtin_amdgcn_raw_buffer_store_b128(v, r, voff, soff, 16); }
__device__ __forceinline__ void st4x(u32x4 v, rsrc_t r, int voff, int soff) { __builtin_amdgcn_raw_buffer_store_b128(v, r, voff, soff, SA); }
__device__ __forceinline__ void relx() {
}
__device__ __forceinline__ void st2(u32x2 v, rsrc_t r, int voff, int soff) { __builtin_amdgcn_raw_buffer_store_b64(v, r, voff, soff, 16); }
template <int CTRL> __device__ __forceinline__ float dpp(float x) {
  return __builtin_bit_cast(float, __builtin_amdgcn_mov_dpp(__builtin_bit_cast(int, x), CTRL, 0xf, 0xf, true));
}
template <int CTRL> __device__ __forceinline__ unsigned dppu(unsigned x) { return (unsigned)__builtin_amdgcn_mov_dpp((int)x, CTRL, 0xf, 0xf, true); }
__device__ __forceinline__ unsigned cvtpk(float lo, float hi) { f32x2 v = {lo, hi}; return __builtin_bit_cast(unsigned, __builtin_convertvector(v, bf16x2)); }
__device__ __forceinline__ float rbf(float x) { return __uint_as_float(cvtpk(x, x) << 16); }
__device__ __forceinline__ float lo16(unsigned v) { return __uint_as_float(v << 16); }
__device__ __forceinline__ float hi16(unsigned v) { return __uint_as_float(v & 0xffff0000u); }
__device__ __forceinline__ float f(unsigned v) { return __uint_as_float(v); }
__device__ __forceinline__ unsigned u(float v) { return __float_as_uint(v); }
__device__ __forceinline__ float dot2(unsigned k, unsigned q, float acc) {
  return __builtin_amdgcn_fdot2_f32_bf16(__builtin_bit_cast(bf16x2, k), __builtin_bit_cast(bf16x2, q), acc, false);
}
__device__ __forceinline__ float xrow_max(float x) {
  auto s = __builtin_amdgcn_permlane16_swap(u(x), u(x), false, false);
  x = fmaxf(f(s[0]), f(s[1]));
  auto t = __builtin_amdgcn_permlane32_swap(u(x), u(x), false, false);
  return fmaxf(f(t[0]), f(t[1]));
}
__device__ __forceinline__ float xrow_sum(float x) {
  auto s = __builtin_amdgcn_permlane16_swap(u(x), u(x), false, false);
  x = f(s[0]) + f(s[1]);
  auto t = __builtin_amdgcn_permlane32_swap(u(x), u(x), false, false);
  return f(t[0]) + f(t[1]);
}
__device__ __forceinline__ f32x4 rowmma(unsigned pp, const u32x4& msk, u32x4 w, f32x4 acc) {
  u32x4 a = {pp & msk[0], pp & msk[1], pp & msk[2], pp & msk[3]};
  return __builtin_amdgcn_mfma_f32_16x16x32_bf16(__builtin_bit_cast(bf16x8, a), __builtin_bit_cast(bf16x8, w), acc, 0, 0, 0);
}
__device__ __forceinline__ void wg_barrier() {
  __builtin_amdgcn_sched_barrier(0);
  asm volatile("s_waitcnt lgkmcnt(0)\n\ts_barrier" ::: "memory");
  __builtin_amdgcn_sched_barrier(0);
}
__device__ __forceinline__ unsigned bad4(const u32x4& v, unsigned tag) { return (v[1] ^ tag) | (v[3] ^ tag); }
__device__ __forceinline__ void cfence() { asm volatile("" ::: "memory"); }
template <int N> __device__ __forceinline__ void vwait() { asm volatile("s_waitcnt vmcnt(%0)" ::"n"(N) : "memory"); }

template <typename F, int... I> __device__ __forceinline__ void sfor_impl(F&& fn, std::integer_sequence<int, I...>) { (fn(std::integral_constant<int, I>{}), ...); }
template <int N, typename F> __device__ __forceinline__ void sfor(F&& fn) { sfor_impl(fn, std::make_integer_sequence<int, N>{}); }

struct Sched { int pre[65]; int post[65]; };
constexpr Sched make_sched() {
  Sched s{}; int cp = 0;
  for (int t = 0; t < 65; ++t) { s.pre[t] = 0; s.post[t] = 0; }
  for (int t = RING; t < 64; ++t) {
    int c1 = cp > t - RING + 1 ? cp : t - RING + 1;
    int lim = t - PACE + 1;
    int c2 = cp + CATCH < lim ? cp + CATCH : lim;
    if (c2 < c1) c2 = c1;
    s.pre[t] = c1; s.post[t] = c2; cp = c2;
  }
  return s;
}
constexpr Sched SCH = make_sched();

__device__ __forceinline__ void load40(rsrc_t rx, int off, unsigned (&d)[10]) {
  const u32x4 a = bl4<0>(rx, off, 0), b = bl4<0>(rx, off + 16, 0); const u32x2 c = bl2<0>(rx, off + 32, 0);
  d[0] = a[0]; d[1] = a[1]; d[2] = a[2]; d[3] = a[3]; d[4] = b[0]; d[5] = b[1]; d[6] = b[2]; d[7] = b[3]; d[8] = c[0]; d[9] = c[1];
}
__device__ __forceinline__ void load_x(rsrc_t rx, int lane, int j, unsigned (&sd)[10], unsigned (&rd)[10]) {
  const int g = lane >> 4, bb = (lane >> 3) & 1, jj = lane & 7;
  load40(rx, bb * 1280 + (g * 8 + jj) * 40, sd);
  load40(rx, bb * 1280 + j * 160 + g * 40, rd);
}
__device__ __forceinline__ float sum_batch(float v) {
  v += dpp<0xB1>(v); v += dpp<0x4E>(v); v += dpp<0x141>(v);
  return xrow_sum(v);
}
__device__ __forceinline__ void layernorm_rows(const unsigned (&sd)[10], const unsigned (&rd)[10], unsigned (&xa)[20]) {
  float s = 0.f;
#pragma unroll
  for (int i = 0; i < 10; ++i) s += lo16(sd[i]) + hi16(sd[i]);
  const float mean = sum_batch(s) * (1.f / 640.f);
  float q = 0.f;
#pragma unroll
  for (int i = 0; i < 10; ++i) { float a = lo16(sd[i]) - mean, b = hi16(sd[i]) - mean; q = fmaf(a, a, q); q = fmaf(b, b, q); }
  const float var = sum_batch(q) * (1.f / 640.f);
  const float mb = rbf(mean);
  const float den = rbf(__builtin_sqrtf(rbf(rbf(var) + 1e-5f)));
  const float inv = 1.f / den;
#pragma unroll
  for (int i = 0; i < 10; ++i) {
    unsigned d = cvtpk(lo16(rd[i]) - mb, hi16(rd[i]) - mb);
    float a = lo16(d) * inv, b = hi16(d) * inv;
    xa[2 * i] = cvtpk(a, a); xa[2 * i + 1] = cvtpk(b, b);
  }
}


#define ENDP() do { __builtin_amdgcn_s_waitcnt(0); __builtin_amdgcn_endpgm(); } while (0)
__device__ __forceinline__ void pace(unsigned& nd, unsigned inc) {
  asm volatile("1:\n\ts_memrealtime vcc\n\ts_waitcnt lgkmcnt(0)\n\ts_lshl4_add_u32 vcc_lo, vcc_lo, %0\n\ts_cmp_lt_i32 vcc_lo, 0\n\ts_cbranch_scc1 1b\n\ts_sub_u32 %0, %0, %1"
               : "+s"(nd) : "s"(inc) : "vcc", "scc", "memory");
}
__device__ __forceinline__ unsigned origin(unsigned long long ta, unsigned ts) {
  asm volatile("" : "+s"(ta));
  const unsigned t0 = (unsigned)ta;
  const int d = (int)((unsigned)__builtin_amdgcn_s_memrealtime() - t0) - TB0;
  return t0 + (d > 0 ? d : 0);
}
template <int N> __device__ __forceinline__ void delay() {
  if constexpr (N > 127) { __builtin_amdgcn_s_sleep(127); delay<N - 127>(); }
  else if constexpr (N > 0) __builtin_amdgcn_s_sleep(N);
}

__global__ void __launch_bounds__(640, 3) fused_decode(const char*, const char*, const char*, const char*, const char*, const char*, const char*, const char*,
                                                       char*, char*) {
  __shared__ __attribute__((aligned(16))) unsigned s_q[64];
  __shared__ __attribute__((aligned(16))) unsigned s_flag[4];
  __shared__ __attribute__((aligned(16))) unsigned s_ml[16];
  __shared__ __attribute__((aligned(16))) float s_o[8 * 128];
  __shared__ __attribute__((aligned(16))) float s_y[17 * YST];

  asm volatile("s_load_dwordx16 s[64:79], %0, 0x0\n\ts_load_dwordx4 s[80:83], %0, 0x40\n\ts_memrealtime s[84:85]" :: "s"(__builtin_amdgcn_kernarg_segment_ptr())
               : "s64", "s65", "s66", "s67", "s68", "s69", "s70", "s71", "s72", "s73", "s74", "s75", "s76", "s77", "s78", "s79", "s80", "s81", "s82", "s83", "s84", "s85");
  int blk = blockIdx.x;
  const unsigned cpf = bl1<0>(mk((const void*)((__builtin_amdgcn_s_getpc() & ~255ull) + (blk >> 3) * 1024), blk < CPF / 1024 * 8 ? 1024 : 0), threadIdx.x * 16, 0);
  __builtin_amdgcn_sched_barrier(0);
  const int lane = threadIdx.x & 63, wave = __builtin_amdgcn_readfirstlane(threadIdx.x >> 6);
  const int g = lane >> 4, c = lane & 15;
  const f32x4 zero4 = {0.f, 0.f, 0.f, 0.f};
  auto mkmsk = [&]() {
    u32x4 m;
    const int jj = lane & 7; const unsigned hm = (jj & 1) ? 0xffff0000u : 0x0000ffffu;
#pragma unroll
    for (int w = 0; w < 4; ++w) m[w] = ((jj >> 1) == w) ? hm : 0u;
    return m;
  };
#define ROLE_IDS() asm volatile("" : "+s"(blk)); __builtin_assume(blk >= 0 && blk < 256); const int h = (blk & 7) * 4 + (blk >> 6), j = (blk >> 3) & 7
#define TILE_IDS() asm volatile("" : "+s"(blk)); __builtin_assume(blk >= 0 && blk < 256); const bool isO = blk < 160, tile = blk < 230; \
  const int bd = (blk + 96) & 255, dq = (bd * 205) >> 11, hi80 = (blk + 176) >> 8; \
  const int tpo = (blk & 7) * 2 + hi80, tpd = 16 + dq, tlo = (blk >> 3) - 10 * hi80, tld = bd - 10 * dq; \
  const int tp = isO ? tpo : tpd, tl = isO ? tlo : tld; \
  const unsigned wsz0 = tp == 22 ? 192 * 1280 : 0, wsz = tp < 22 ? 256 * 1280 : wsz0, wtb = (tp & 15) * (256 * 1280)
#define WTILE() mk((isO ? wo : wd) + wtb, wsz)
#define KP(v, i) ((char*)(((unsigned long long)v[2 * i + 1] << 32) | v[2 * i]))
#define ARGS() u32x16 ka; u32x4 kb; unsigned long long t64; __builtin_amdgcn_sched_barrier(0); \
  asm volatile("s_waitcnt lgkmcnt(0)" : "={s[64:79]}"(ka), "={s[80:83]}"(kb), "={s[84:85]}"(t64)); \
  const char *x = KP(ka, 0), *K = KP(ka, 1), *V = KP(ka, 2), *wq = KP(ka, 3), *wo = KP(ka, 4), *wg = KP(ka, 5), *wu = KP(ka, 6), *wd = KP(ka, 7); \
  char *out = KP(kb, 0), *ws = KP(kb, 1); const unsigned long long ta = TA ? __builtin_amdgcn_s_memrealtime() : t64; const unsigned t0 = (unsigned)t64; const rsrc_t rws = mk(ws, WS_END)

  if (wave == 0) {
    __builtin_amdgcn_s_setprio(3);
    ROLE_IDS();
    int xo0 = ((lane >> 3) & 1) * 1280 + (g * 8 + (lane & 7)) * 40, xo1 = ((lane >> 3) & 1) * 1280 + j * 160 + g * 40, wqo = g * (20 * 8192) + c * 16;
    int wqb = j * (80 * 8192) + h * 256;
    asm volatile("" : "+v"(xo0), "+v"(xo1), "+v"(wqo), "+s"(wqb));
    ARGS();
    const unsigned E = bl1<16>(rws, 0, 0);
    unsigned sd[10], rd[10];
    load40(mk(x, 2560), xo0, sd); load40(mk(x, 2560), xo1, rd);
    const rsrc_t rq = mk(wq + (unsigned)wqb, 80 * 8192);
    u32x4 w[20];
#pragma unroll
    for (int i = 0; i < 20; ++i) { w[i] = bl4<QAUX>(rq, wqo, i * 8192); __builtin_amdgcn_sched_barrier(0); }
    if (lane == 0) s_flag[0] = 0;
    if (lane < 16) s_ml[lane] = 0;
    wg_barrier();
    const unsigned tz = origin(ta, t0);
    const u32x4 msk = mkmsk();
    unsigned xa[20];
    layernorm_rows(sd, rd, xa);
    const unsigned tag = __builtin_amdgcn_readfirstlane(E) * 1664525u + 1013904223u;
    f32x4 acc = zero4;
#pragma unroll
    for (int i = 0; i < 20; ++i) acc = rowmma(xa[i], msk, w[i], acc);
    {
      const int o = h * 16384 + (g >> 1) * 8192 + j * 1024 + ((g & 1) * 16 + c) * 16;
      u32x4 v0 = {u(acc[0]), tag, u(acc[1]), tag}, v1 = {u(acc[2]), tag, u(acc[3]), tag};
      st4x(v0, rws, o, WS_QP); st4x(v1, rws, o + 512, WS_QP); relx();
      if (blk < 8 && lane < 32) { u32x2 tb = {tz, tag}; st2(tb, rws, lane * 128 + blk * 8, WS_TB); }
    }
    delay<DQ>();
    float q0, q1;
    const u32x2 tb = bl2<16>(rws, h * 128 + (lane & 7) * 8, WS_TB);
    int spin = 0;
    for (;; ++spin) {
      cfence();
      u32x4 v[8];
#pragma unroll
      for (int p = 0; p < QN; ++p) v[p] = bl4<16>(rws, h * 16384 + (j >> 2) * 8192 + lane * 16 + p * 1024, WS_QP);
      unsigned bad = 0; q0 = 0.f; q1 = 0.f;
#pragma unroll
      for (int p = 0; p < QN; ++p) { bad |= bad4(v[p], tag); q0 += f(v[p][0]); q1 += f(v[p][2]); }
      if (__all(bad == 0) || spin >= SPIN_MAX) break;
      __builtin_amdgcn_s_sleep(16);
    }
    s_q[4 * c + 2 * (g & 1) + (g >> 1)] = cvtpk(q0, q1);
    if (lane == 0) s_flag[1] = tag;
    {
      int d = tb[1] == tag ? (int)(tb[0] - tz) : 0;
      d = d < -300 ? -300 : (d > 300 ? 300 : d);
      int mx = tb[1] == tag ? d : -300;
      mx = max(mx, (int)dppu<0xB1>(mx)); mx = max(mx, (int)dppu<0x4E>(mx)); mx = max(mx, (int)dppu<0x141>(mx));
      d += (int)dppu<0xB1>(d); d += (int)dppu<0x4E>(d); d += (int)dppu<0x141>(d);
      d += max(mx - (d >> 3) - CZ, 0) * CK;
      if (lane == 0) s_flag[2] = tz + (d >> 3);
    }
    cfence();
    if (lane == 0) *(lds_vu32*)&s_flag[0] = 1;
    unsigned mw, lw;
    for (;;) {
      lw = *(lds_vu32*)&s_ml[2 * (lane & 7) + 1];
      if (__all(lw != 0)) break;
      __builtin_amdgcn_s_sleep(4);
    }
    mw = *(lds_vu32*)&s_ml[2 * (lane & 7)];
    float Mb = fmaxf(f(mw), dpp<0xB1>(f(mw))); Mb = fmaxf(Mb, dpp<0x4E>(Mb)); Mb = fmaxf(Mb, dpp<0x141>(Mb));
    float Lb = __builtin_amdgcn_exp2f((f(mw) - Mb) * L2E) * f(lw);
    Lb += dpp<0xB1>(Lb); Lb += dpp<0x4E>(Lb); Lb += dpp<0x141>(Lb);
    if (lane == 0) { u32x4 ml = {u(Mb), tag, u(Lb), tag}; st4(ml, rws, (h * 8 + j) * 128, WS_ML); }
    delay<DM>();
    u32x4 ml = {0u, tag, 0u, tag};
    bool need = lane < 4;
    for (spin = 0;; ++spin) {
      cfence();
      if (need) ml = bl4<16>(rws, (h * 8 + (j & 4) + lane) * 128, WS_ML);
      need = bad4(ml, tag) != 0;
      if (!__any(need) || spin >= SPIN_MAX) break;
      __builtin_amdgcn_s_sleep(16);
    }
    float M = fmaxf(f(ml[0]), dpp<0xB1>(f(ml[0]))); M = fmaxf(M, dpp<0x4E>(M));
    float L = __builtin_amdgcn_exp2f((f(ml[0]) - M) * L2E) * f(ml[2]);
    L += dpp<0xB1>(L); L += dpp<0x4E>(L);
    M = f(__builtin_amdgcn_readfirstlane(u(M))); L = f(__builtin_amdgcn_readfirstlane(u(L)));
    const float fw = __builtin_amdgcn_exp2f((f(mw) - M) * L2E) / L;
    float fs[8];
#pragma unroll
    for (int w2 = 0; w2 < 8; ++w2) fs[w2] = f(__builtin_amdgcn_readlane(u(fw), w2));
    TILE_IDS();
    asm volatile("" :: "v"(cpf));
    unsigned lo = (unsigned)(size_t)(lds_f32x2*)&s_o[2 * lane];
    unsigned ly = (unsigned)(size_t)(lds_f32x2*)&s_y[(lane >> 5) * 64 + ((lane >> 1) & 1) * 32 + ((lane >> 2) & 7) * 4 + 2 * (lane & 1)];
    int go = WS_OP + (h * 8 + j) * 1024 + lane * 8, gy = WS_YP + (tl * 24 + tp) * 1024 + lane * 16;
    u32x4 v = {0u, tag, 0u, tag};
    asm volatile("" : "+v"(lo), "+v"(ly), "+v"(go), "+v"(gy), "+v"(v));
    const rsrc_t rwp = rws;
    wg_barrier();
    {
      f32x2 ov[8];
#pragma unroll
      for (int w2 = 0; w2 < 8; ++w2) ov[w2] = ((const lds_f32x2*)(size_t)lo)[w2 * 64];
      float a0 = fs[0] * ov[0][0], a1 = fs[0] * ov[0][1];
#pragma unroll
      for (int w2 = 1; w2 < 8; ++w2) { a0 = fmaf(fs[w2], ov[w2][0], a0); a1 = fmaf(fs[w2], ov[w2][1], a1); }
      st2(u32x2{cvtpk(a0, a1), v[1]}, rwp, go, 0);
    }
    if (tile) {
      wg_barrier();
      f32x2 yv[NYP];
#pragma unroll
      for (int w2 = 0; w2 < NYP; ++w2) yv[w2] = ((const lds_f32x2*)(size_t)ly)[w2 * (YST / 2)];
#pragma unroll
      for (int d = 1; d < NYP / 2; d <<= 1)
#pragma unroll
        for (int w2 = 0; w2 < NYP; w2 += 2 * d) yv[w2] += yv[w2 + d];
      unsigned y0, y1;
      asm("v_add_f32 %0, %2, %3\n\tv_add_f32 %1, %4, %5" : "=&v"(y0), "=v"(y1) : "v"(yv[0][0]), "v"(yv[NYP / 2][0]), "v"(yv[0][1]), "v"(yv[NYP / 2][1]));
      v[0] = y0; v[2] = y1;
      st4(v, rwp, gy, 0);
      if (NC2 > 1) st4(v, rwp, gy, YC1);
      if (NC2 > 2) st4(v, rwp, gy, YC2);
    }
    __builtin_amdgcn_endpgm();
  } else if (wave == 1) {
    __builtin_amdgcn_s_setprio(2);
    ROLE_IDS();
    const int hf0 = (blk & 7) * 3 + (blk >> 6), hf1 = 24 + ((blk - j) & 7), hf = blk < 192 ? hf0 : hf1;
    TILE_IDS();
    constexpr int INV = 0x7ff00000;
    int xo0 = ((lane >> 3) & 1) * 1280 + (g * 8 + (lane & 7)) * 40, xo1 = ((lane >> 3) & 1) * 1280 + j * 160 + g * 40;
    const int ocol = ((blk >> 2) & 1) * 640 + ((blk - 176) >> 3) * 64 + (blk & 3) * 16 + 2 * (lane & 7);
    int xo2 = blk >= 176 ? ocol * 2 : 0;
    int po = INV, pw = INV, pg = INV, pv0 = lane == 0 ? 0 : INV, pz = blk == 0 ? lane * 64 : INV;
    unsigned vbase = ((j >> 2) * 32 + h) << 20;
    {
      const int bi = h * 8 + j, fi = hf * 8 + j;
      const int bq = WS_QP + bi * 2048, bf0 = WS_FP + fi * 2048 - 16 * 128, bf = hf < 27 ? bf0 : INV;
      const int by0 = WS_YP + (tl * 24 + tp) * 1024 - 32 * 128, by = tile ? by0 : INV, bo = WS_OP + bi * 1024 - 48 * 128;
      const int bh = WS_HP + fi * 128 - 56 * 128, bm = WS_ML + bi * 128 - 57 * 128, bt0 = WS_TB + h * 128 - 58 * 128, bt = j == 0 ? bt0 : INV;
      po = lane == 58 ? bt : po; po = lane == 57 ? bm : po; po = lane == 56 ? bh : po; po = lane < 52 ? bo : po;
      po = lane < 48 ? INV : po; po = lane < 40 ? by : po;
      if (NOPT & 1) po = lane < 32 ? INV : po; else { po = lane < 32 ? bf : po; po = lane < 16 ? bq : po; }
      po += lane * 128;
      const int w0 = (lane * 191 + (lane >> 1) * -127) * 1280 + tl * 128, g0 = (80 * j + 79 * lane) * 3456 + hf * 128, g1 = hf < 27 ? g0 : INV;
      pw = lane < 3 ? w0 : pw; pg = lane < 2 ? g1 : pg;
    }
    asm volatile("" : "+v"(xo0), "+v"(xo1), "+v"(xo2), "+v"(po), "+v"(pw), "+v"(pg), "+v"(pv0), "+v"(pz), "+s"(vbase));
    ARGS();
    const unsigned E = bl1<16>(rws, 0, 0);
    const rsrc_t rx = mk(x, 2560);
    unsigned sd[10], rd[10];
    load40(rx, xo0, sd); load40(rx, xo1, rd);
    const unsigned xres = bl1<0>(rx, xo2, 0);
    unsigned pt0 = bl1<16>(rws, po, 0), pt1 = bl1<16>(rws, po, 64);
    if (!(LT & 1)) pt1 ^= bl1<2>(mk(V + vbase, 4), pv0, 0);
    if (!(LT & 2)) pt1 ^= bl1<PWA>(WTILE(), pw, 0);
    pt1 ^= bl1<0>(mk(wg, 640 * 3456), pg, 0);
    pt1 ^= bl1<0>(mk(wu, 640 * 3456), pg, 0);
    if (!(LT & 4)) pt1 ^= bl1<16>(mk(out, 2560), pz, 0);
    wg_barrier();
    unsigned tag;
    const u32x4 msk = mkmsk();
    if (hf < 27) {
      const gptr p = (gptr)(c < 8 ? wg : wu) + (size_t)((80 * j + 20 * g) * 3456 + hf * 128 + (c & 7) * 16);
      u32x4 w[20];
#pragma unroll
      for (int i = 0; i < 20; ++i) {
        w[i] = __builtin_nontemporal_load((__attribute__((address_space(1))) const u32x4*)(p + i * 3456));
        __builtin_amdgcn_sched_barrier(0);
      }
      __builtin_amdgcn_sched_barrier(0);
      unsigned xa[20];
      layernorm_rows(sd, rd, xa);
      tag = __builtin_amdgcn_readfirstlane(E) * 1664525u + 1013904223u;
      f32x4 acc = zero4;
#pragma unroll
      for (int i = 0; i < 20; ++i) acc = rowmma(xa[i], msk, w[i], acc);
      {
        const int o = hf * 16384 + (c & 7) * 2048 + j * 256 + ((c >> 3) * 4 + g) * 16;
        u32x4 v0 = {u(acc[0]), tag, u(acc[1]), tag}, v1 = {u(acc[2]), tag, u(acc[3]), tag};
        st4x(v0, rws, o, WS_FP); st4x(v1, rws, o + 128, WS_FP); relx();
      }
      delay<DF>();
      float a0, a1;
      int spin = 0;
      for (;; ++spin) {
        cfence();
        const u32x4 v0 = bl4<16>(rws, hf * 16384 + j * 2048 + lane * 16, WS_FP), v1 = bl4<16>(rws, hf * 16384 + j * 2048 + lane * 16 + 1024, WS_FP);
        const bool ok = (bad4(v0, tag) | bad4(v1, tag)) == 0;
        a0 = f(v0[0]) + f(v1[0]); a1 = f(v0[2]) + f(v1[2]);
        if (__all(ok) || spin >= SPIN_MAX) break;
        __builtin_amdgcn_s_sleep(32);
      }
      a0 = rbf(xrow_sum(a0)); a1 = rbf(xrow_sum(a1));
      const float u0 = dpp<0x104>(a0), u1 = dpp<0x104>(a1);
      auto gelu = [](float v) {
        const float z = 0.7978845608028654f * (v + 0.044715f * v * v * v);
        const float t = 1.f - 2.f / (1.f + __builtin_amdgcn_exp2f(2.f * L2E * z));
        return 0.5f * v * (1.f + t);
      };
      const unsigned hp = cvtpk(rbf(gelu(a0)) * u0, rbf(gelu(a1)) * u1);
      if (lane < 16 && (c & 4) == 0) {
        const int gp = c & 3, st = c >> 3;
        u32x2 v = {hp, tag};
        st2(v, rws, (hf * 8 + j) * 128 + ((gp >> 1) * 4 + 2 * (gp & 1) + st) * 8, WS_HP);
      }
    } else {
      tag = __builtin_amdgcn_readfirstlane(E) * 1664525u + 1013904223u;
    }
    if (LT) while (__builtin_amdgcn_readfirstlane(*(lds_vu32*)&s_flag[0]) == 0) __builtin_amdgcn_s_sleep(8);
    if (LT & 1) pt1 ^= bl1<2>(mk(V + vbase, 4), pv0, 0);
    if (LT & 2) pt1 ^= bl1<PWA>(WTILE(), pw, 0);
    if (LT & 4) pt1 ^= bl1<16>(mk(out, 2560), pz, 0);
    if (NC2 > 1) pt0 ^= bl1<16>(rws, lane < 16 * (NC2 - 1) && tile ? WS_YP + CS + (lane >> 4) * CS + (tl * 24 + tp) * 1024 + (lane & 15) * 64 : INV, 0);
    asm volatile("" :: "v"(cpf));
    asm volatile("" :: "v"(pt0), "v"(pt1), "s"(tag));
    wg_barrier();
    unsigned nr = 0u - ((unsigned)__builtin_amdgcn_s_memrealtime() + TRD) * 16u;
    unsigned nrg = 0u - (__builtin_amdgcn_readfirstlane(s_flag[2]) + TOFF + (RING * DT + (64 - RING) * DT2) / 10 + TRX) * 16u;
    if (tile) wg_barrier();
    if (blk >= 176) {
      const int yo = (((blk - 176) >> 3) * 24 + (lane >> 3)) * 1024 + (blk & 7) * 128 + (lane & 7) * 16;
      const int yo2 = lane < 56 ? yo + 16384 : 0x7ff00000;
      unsigned tag2 = lane < 56 ? tag : 0u;
      int oo = lane < 8 ? ocol * 2 : 0x7ff00000;
      float x0 = lo16(xres), x1 = hi16(xres);
      const rsrc_t rout = mk(out, 2560);
      asm volatile("" : "+v"(oo), "+v"(x0), "+v"(x1), "+v"(tag2));
      pace(nr, 0);
      pace(nrg, 0);
      u32x4 y[3][3], v0, v1, v2;
      unsigned n0 = 1, n1 = 1, n2 = 1;
      auto rd = [&](auto K) { constexpr int k = K; constexpr unsigned o = WS_YP + (k == 0 ? 0 : k == 1 ? YC1 : YC2);
        y[k][0] = bl4<16>(rws, n0 ? yo : INV, o); y[k][1] = bl4<16>(rws, n1 ? yo : INV, o + 8192); y[k][2] = bl4<16>(rws, n2 ? yo2 : INV, o); };
      auto stage = [&](auto K, bool last) { constexpr int k = K;
        v0 = n0 ? y[k][0] : v0; v1 = n1 ? y[k][1] : v1; v2 = n2 ? y[k][2] : v2;
        float a0 = f(v0[0]) + f(v1[0]) + f(v2[0]), a1 = f(v0[2]) + f(v1[2]) + f(v2[2]);
        auto t = __builtin_amdgcn_permlane32_swap(u(a0), u(a1), false, false);
        float a = f(t[0]) + f(t[1]);
        a += dpp<0x128>(a);
        auto t2 = __builtin_amdgcn_permlane16_swap(u(a), u(a), false, false);
        a = f(t2[0]) + f(t2[1]);
        auto t3 = __builtin_amdgcn_permlane32_swap(u(a), u(a), false, false);
        const unsigned r = cvtpk(x0 + f(t3[0]), x1 + f(t3[1]));
        n0 = bad4(v0, tag); n1 = bad4(v1, tag); n2 = bad4(v2, tag2);
        unsigned nb = n0 | n1 | n2;
        asm volatile("" : "+v"(nb));
        const bool again = __builtin_amdgcn_ballot_w64(nb != 0) != 0 && !last;
        __builtin_amdgcn_raw_buffer_store_b32(r, mk(out, again ? 0 : 2560), oo, 0, OAUX);
        __builtin_amdgcn_sched_barrier(0);
        return again; };
      const std::integral_constant<int, 0> k0; const std::integral_constant<int, 1> k1; const std::integral_constant<int, 2> k2;
      rd(k0);
      __builtin_amdgcn_sched_barrier(0);
      nr = 0u - ((unsigned)__builtin_amdgcn_s_memrealtime() + G2 - 2) * 16u;
      pace(nr, G3 * 16);
      rd(k1);
      pace(nr, 0);
      rd(k2);
      __builtin_amdgcn_sched_barrier(0);
      int spin = 0;
      if (stage(k0, false)) {
        spin = 1;
        rd(k0);
        __builtin_amdgcn_sched_barrier(0);
        if (stage(k1, false)) {
          spin = 2;
          rd(k1);
          __builtin_amdgcn_sched_barrier(0);
          if (stage(k2, false)) {
            spin = 3;
            rd(k2);
            __builtin_amdgcn_sched_barrier(0);
            if (stage(k0, false)) {
              spin = 4;
              rd(k0);
              __builtin_amdgcn_sched_barrier(0);
              if (stage(k1, false)) {
                spin = 5;
                rd(k1);
                __builtin_amdgcn_sched_barrier(0);
                if (stage(k2, false)) {
                  spin = 6;
                  if (stage(k0, false)) {
                    spin = 7;
                    while (stage(k1, spin >= SPIN_MAX)) { ++spin; cfence(); rd(k1); }
                  }
                }
              }
            }
          }
        }
      }
      if (blk == 176 && lane == 0) __builtin_amdgcn_raw_buffer_store_b32(tag, rws, 0, 0, 16);
    }
    __builtin_amdgcn_endpgm();
  } else {
    ROLE_IDS();
    const int sw = wave - 2;
    unsigned kvbase = ((j >> 2) * 32 + h) << 20;
    const int wi = (j & 3) * 8 + sw;
    constexpr int KSTEP = 1024 << PAT;
    const int vb = (wi >> PAT) * (32768 << PAT) + (wi & ((1 << PAT) - 1)) * 1024;
    const int vv = vb + lane * 16;
    int vk = vb + (c >> 2) * 256 + g * 64 + (c & 3) * 16;
    asm volatile("" : "+v"(vk), "+s"(kvbase));
    ARGS();
    const rsrc_t rk = mk(K + kvbase, 1 << 20), rv = mk(V + kvbase, 1 << 20);
    u32x4 buf[64];
    unsigned inc = DT * 16 / 10, nd = 0, tz = 0;
    auto issue = [&](auto T) { constexpr int t = T;
      constexpr int so = (t & 31) * KSTEP;
      buf[t] = t < 32 ? bl4<2>(rk, vk, so) : bl4<2>(rv, vv, so); };
    sfor<RING>([&](auto T) {
      constexpr int t = T;
      if constexpr (t == BAR0) { wg_barrier(); tz = origin(ta, t0); nd = 0u - (tz * 16u + (TOFF * 10 + NFREE * DT) * 16 / 10); }
      if constexpr (t >= PACE) vwait<PACE - 1>();
      if constexpr (t >= NFREE) pace(nd, inc);
      issue(T);
      __builtin_amdgcn_sched_barrier(0);
    });
    asm volatile("" :: "v"(cpf));
    TILE_IDS();
    const u32x4 msk = mkmsk();
    while (__builtin_amdgcn_readfirstlane(*(lds_vu32*)&s_flag[0]) == 0) __builtin_amdgcn_s_sleep(1);
    cfence();
    u32x4 qb[4];
    {
      const u32x4 qp = *(const u32x4*)&s_q[16 * g + 4 * (c & 3)], z = {0u, 0u, 0u, 0u};
#pragma unroll
      for (int i = 0; i < 4; ++i) qb[i] = (c >> 2) == i ? qp : z;
    }
    const unsigned tag = __builtin_amdgcn_readfirstlane(s_flag[1]);
    inc = DT2 * 16 / 10 + (unsigned)(((int)(__builtin_amdgcn_readfirstlane(s_flag[2]) - tz) * (16384 / (64 - RING))) >> 10);
    float sc[8];
    unsigned pp[8];
    f32x4 acc = zero4, sa = zero4;
    auto consume = [&](auto I) {
      constexpr int i = I;
      if constexpr (i < 32) {
        sa = __builtin_amdgcn_mfma_f32_16x16x32_bf16(__builtin_bit_cast(bf16x8, buf[i]), __builtin_bit_cast(bf16x8, qb[i & 3]), (i & 3) ? sa : zero4, 0, 0, 0);
        if constexpr ((i & 3) == 3) {
          const float d0 = (c & 1) ? sa[1] : sa[0], d1 = (c & 1) ? sa[3] : sa[2];
          float d = (c & 2) ? d1 : d0;
          d += dpp<0xB1>(d); d += dpp<0x4E>(d);
          sc[i >> 2] = rbf(rbf(d) * 0.08838834764831845f);
        }
        if constexpr (i == 31) {
          float m = fmaxf(fmaxf(fmaxf(sc[0], sc[1]), fmaxf(sc[2], sc[3])), fmaxf(fmaxf(sc[4], sc[5]), fmaxf(sc[6], sc[7])));
          m = fmaxf(m, dpp<0x124>(m)); m = fmaxf(m, dpp<0x128>(m));
          m = xrow_max(m);
          const float mb = m * L2E;
          float l = 0.f;
#pragma unroll
          for (int k = 0; k < 8; ++k) { const float p = __builtin_amdgcn_exp2f(fmaf(sc[k], L2E, -mb)); l += p; pp[k] = cvtpk(p, p); }
          l += dpp<0x124>(l); l += dpp<0x128>(l);
          l = xrow_sum(l);
          if (lane == 0) { u32x2 v = {u(m), u(l)}; *(u32x2*)&s_ml[2 * sw] = v; }
        }
      } else {
        acc = rowmma(dppu<0x150 + 4 * (i & 3)>(pp[(i - 32) >> 2]), msk, buf[i], acc);
      }
    };
    u32x4 wv[4], hb;
    const rsrc_t rw = WTILE();
    const int vw = (32 * sw + (c >> 3) * 4 + g) * 1280 + tl * 128 + (c & 7) * 16;
    auto wload = [&](auto I) { constexpr int i = I; wv[i] = bl4<(WNT >> i) & 1 ? 2 : 0>(rw, vw, i * 8 * 1280); };
    const int hi = (tp - 16) * 32 + 4 * sw + (c & 3);
    int pa = isO ? WS_OP + ((2 * tp + (sw >> 2)) * 8 + (c & 4) + g) * 1024 + (sw & 3) * 128 + (c >> 3) * 64 + (c & 3) * 16
                 : hi < 216 ? WS_HP + hi * 128 + (c & 4) * 8 + (c >> 3) * 16 : 0x7ff00000;
    asm volatile("" : "+v"(pa));
    unsigned m0, m1, ly, lo;
    sfor<64 - RING>([&](auto T0) {
      constexpr int t = T0 + RING;
      constexpr int c0 = t == RING ? 0 : SCH.post[t - 1], c1 = SCH.pre[t], c2 = SCH.post[t];
      sfor<c1 - c0>([&](auto I) { consume(std::integral_constant<int, c0 + I>{}); });
      __builtin_amdgcn_sched_barrier(0);
      vwait<PACE - 1>();
      pace(nd, inc);
      issue(std::integral_constant<int, t>{});
      if constexpr (t == HT) hb = bl4<16>(mk(ws, isO ? 0 : WS_END), pa, 0);
      if constexpr (t == 60) {
        m0 = msk[0] | msk[2]; m1 = msk[1] | msk[3];
        ly = (unsigned)(size_t)(lds_f32x4*)&s_y[(c < 8 ? sw : DUMP) * YST + (g & 1) * 64 + (g >> 1) * 32 + (c & 7) * 4];
        lo = (unsigned)(size_t)(lds_f32x4*)&s_o[sw * 128 + (c >> 2) * 32 + (g & 1) * 16 + (c & 3) * 4];
        asm volatile("" : "+v"(m0), "+v"(m1), "+v"(ly), "+v"(lo));
      }
      __builtin_amdgcn_sched_barrier(0);
      sfor<c2 - c1>([&](auto I) { consume(std::integral_constant<int, c1 + I>{}); });
      __builtin_amdgcn_sched_barrier(0);
    });
    constexpr int CE = SCH.post[63];
    unsigned ngg = nd - GG * 16;
    sfor<64 - CE>([&](auto I) {
      constexpr int i = I;
      consume(std::integral_constant<int, CE + i>{});
      __builtin_amdgcn_sched_barrier(0);
      if constexpr (CE + i >= 61 && CE + i < 63) { pace(nd, WDT * 16 / 10); wload(std::integral_constant<int, CE + i - 61>{}); }
      __builtin_amdgcn_sched_barrier(0);
    });
    if (lane < 32) ((lds_f32x4*)(size_t)lo)[0] = acc;
    __builtin_amdgcn_sched_barrier(0);
    wload(std::integral_constant<int, 2>{});
    wg_barrier();
    int spin = 0;
    unsigned ng = 0u - ((unsigned)__builtin_amdgcn_s_memrealtime() + D1T) * 16u;
    if (tile) {
      { unsigned n3 = ng + (D1T - W3D) * 16; pace(n3, 0); }
      wload(std::integral_constant<int, 3>{});
      __builtin_amdgcn_sched_barrier(0);
      auto ywrite = [&](f32x4 y0, f32x4 y1) {
#pragma unroll
        for (int k = 0; k < 4; ++k) { auto t = __builtin_amdgcn_permlane32_swap(u(y0[k]), u(y1[k]), false, false); y0[k] = f(t[0]) + dpp<0x128>(f(t[1])); }
        ((lds_f32x4*)(size_t)ly)[0] = y0;
      };
      if (isO) {
        pace(ng, 0);
        pace(ngg, 0);
        u32x4 oa = bl4<16>(rws, pa, 0);
        __builtin_amdgcn_sched_barrier(0);
#pragma unroll
        for (int i = 0; i < 4; i += 2) {
          unsigned x2 = wv[i][2], x3 = wv[i][3], y0 = wv[i + 1][0], y1 = wv[i + 1][1];
          asm volatile("v_swap_b32 %0, %2\n\tv_swap_b32 %1, %3" : "+v"(x2), "+v"(x3), "+v"(y0), "+v"(y1));
          wv[i][2] = x2; wv[i][3] = x3; wv[i + 1][0] = y0; wv[i + 1][1] = y1;
        }
        __builtin_amdgcn_sched_barrier(0);
        for (;; ++spin) {
          const auto d = __builtin_amdgcn_permlane32_swap(oa[0], oa[2], false, false);
          const auto e = __builtin_amdgcn_permlane16_swap(__builtin_amdgcn_perm(d[1], d[0], 0x05040100), __builtin_amdgcn_perm(d[1], d[0], 0x07060302), false, false);
          const float o = dot2(e[0], 0x3f803f80u, dot2(e[1], 0x3f803f80u, 0.f));
          const unsigned p = cvtpk(o, o);
          const u32x4 a0 = {dppu<0x00>(p) & m0, dppu<0x00>(p) & m1, dppu<0x55>(p) & m0, dppu<0x55>(p) & m1};
          f32x4 y0 = __builtin_amdgcn_mfma_f32_16x16x32_bf16(__builtin_bit_cast(bf16x8, a0), __builtin_bit_cast(bf16x8, wv[0]), zero4, 0, 0, 0);
          f32x4 y1 = __builtin_amdgcn_mfma_f32_16x16x32_bf16(__builtin_bit_cast(bf16x8, a0), __builtin_bit_cast(bf16x8, wv[1]), zero4, 0, 0, 0);
          __builtin_amdgcn_sched_barrier(0);
          const u32x4 a1 = {dppu<0xAA>(p) & m0, dppu<0xAA>(p) & m1, dppu<0xFF>(p) & m0, dppu<0xFF>(p) & m1};
          y0 = __builtin_amdgcn_mfma_f32_16x16x32_bf16(__builtin_bit_cast(bf16x8, a1), __builtin_bit_cast(bf16x8, wv[2]), y0, 0, 0, 0);
          y1 = __builtin_amdgcn_mfma_f32_16x16x32_bf16(__builtin_bit_cast(bf16x8, a1), __builtin_bit_cast(bf16x8, wv[3]), y1, 0, 0, 0);
          __builtin_amdgcn_sched_barrier(0);
          const bool again = (__builtin_amdgcn_ballot_w64(oa[1] != tag) | __builtin_amdgcn_ballot_w64(oa[3] != tag)) != 0 && spin < SPIN_MAX;
          __builtin_amdgcn_sched_barrier(0);
          ywrite(y0, y1);
          if (__builtin_expect(!again, 1)) break;
          delay<S1>();
          cfence();
          oa = bl4<16>(rws, pa, 0);
        }
      } else {
        const unsigned taga = hi < 216 ? tag : 0u;
        if (__builtin_expect(__builtin_amdgcn_ballot_w64(bad4(hb, taga) != 0) != 0, 0)) {
          do { delay<S1>(); cfence(); hb = bl4<16>(rws, pa, 0); } while (__builtin_amdgcn_ballot_w64(bad4(hb, taga) != 0) != 0 && ++spin < SPIN_MAX);
        }
        const unsigned p = __builtin_amdgcn_perm(hb[2], hb[0], 0x01000100u + g * 0x02020202u);
        const unsigned pv[4] = {dppu<0x00>(p), dppu<0x55>(p), dppu<0xAA>(p), dppu<0xFF>(p)};
        f32x4 y0 = zero4, y1 = zero4;
#pragma unroll
        for (int i = 0; i < 4; ++i) {
          const u32x2 a = {pv[i] & m0, pv[i] & m1}, b0 = {wv[i][0], wv[i][1]}, b1 = {wv[i][2], wv[i][3]};
          y0 = __builtin_amdgcn_mfma_f32_16x16x16bf16_1k(__builtin_bit_cast(s16x4, a), __builtin_bit_cast(s16x4, b0), y0, 0, 0, 0);
          y1 = __builtin_amdgcn_mfma_f32_16x16x16bf16_1k(__builtin_bit_cast(s16x4, a), __builtin_bit_cast(s16x4, b1), y1, 0, 0, 0);
        }
        ywrite(y0, y1);
      }
      wg_barrier();
    }
    ENDP();
  }
}

extern "C" void kernel_launch(void* const* d_in, const int* in_sizes, int n_in, void* d_out, int out_size, void* d_ws, size_t ws_size, hipStream_t stream) {
  fused_decode<<<256, 640, 0, stream>>>((const char*)d_in[0], (const char*)d_in[1], (const char*)d_in[2], (const char*)d_in[3], (const char*)d_in[4],
                                        (const char*)d_in[6], (const char*)d_in[7], (const char*)d_in[8], (char*)d_out, (char*)d_ws);
}
```
